```python
import torch

def _softmax(x, axis=-1):
    return torch.softmax(x, dim=axis)
def _gelu(x):
    return torch.nn.functional.gelu(x, approximate="tanh")
def _silu(x):
    return torch.nn.functional.silu(x)
def _sigmoid(x):
    return torch.sigmoid(x)
def _layernorm(x, axis=-1, eps=1e-5):
    m = x.mean(dim=axis, keepdim=True)
    v = x.var(dim=axis, keepdim=True, unbiased=False)
    return (x - m) / torch.sqrt(v + eps)
def _scatter_add(tgt, idx, src, axis=0):
    return tgt.clone().index_add_(axis, idx, src)
def _segment_sum(src, seg, n):
    out = torch.zeros((n,) + src.shape[1:], dtype=src.dtype)
    return out.index_add_(0, seg, src)
def _pad(x, pw):
    flat = tuple(v for pair in reversed(pw) for v in pair)
    return torch.nn.functional.pad(x, flat)

kernel_name = 'parallel_sum__sliding_block_cached__dense_activation_silu__decode'

def setup_inputs():

    _w = (lambda a: (lambda t: t.to(torch.bfloat16) if t.is_floating_point() else t)(torch.as_tensor(a)))
    def _A_setup():
        B, Sq, D = 8, 1, 512
        Hq, Hkv, Sk, Dh = 32, 8, 4096, 128
        assert Hq % Hkv == 0, (Hq, Hkv)
        return {
            "x": _w((lambda *s: torch.randn(*s, dtype=torch.bfloat16) * 0.05)(B, Sq, D)),
            "k_cache": _w((lambda *s: torch.randn(*s, dtype=torch.bfloat16) * 3.2)(B, Hkv, Sk, Dh)),
            "v_cache": _w((lambda *s: torch.randn(*s, dtype=torch.bfloat16) * 0.8)(B, Hkv, Sk, Dh)),
            "wq": _w((lambda *s: torch.randn(*s, dtype=torch.bfloat16) * 3.2)(D, Hq * Dh) * (D ** -0.5)),
            "wo": _w((lambda *s: torch.randn(*s, dtype=torch.bfloat16) * 0.8)(Hq * Dh, D) * ((Hq * Dh) ** -0.5)),
        }
    _A_in = _A_setup()

    def _B_setup():
        B, S, D, F = 8, 1, 512, 1792
        return {
            "x": _w((lambda *s: torch.randn(*s, dtype=torch.bfloat16) * 0.05)(B, S, D)),
            "w1": _w((lambda *s: torch.randn(*s, dtype=torch.bfloat16) * 3.2)(D, F) * (D ** -0.5)),
            "w2": _w((lambda *s: torch.randn(*s, dtype=torch.bfloat16) * 0.4)(F, D) * (F ** -0.5)),
        }
    _B_in = _B_setup()

    return {
        "A_x": _A_in["x"],
        "A_k_cache": _A_in["k_cache"],
        "A_v_cache": _A_in["v_cache"],
        "A_wq": _A_in["wq"],
        "A_wo": _A_in["wo"],
        "B_x": _B_in["x"],
        "B_w1": _B_in["w1"],
        "B_w2": _B_in["w2"],
    }

def reference(A_x, A_k_cache, A_v_cache, A_wq, A_wo, B_x, B_w1, B_w2):
    def _A_ref(x, k_cache, v_cache, wq, wo):
        B, Sq, D = x.shape
        Hq, Dh = 32, 128
        Hkv, Sk = k_cache.shape[1], k_cache.shape[2]
        W = min(4096, max(256, Sk // 4))
        rep = Hq // Hkv
        q = (lambda x, *d: x.permute(*d))((x @ wq).reshape(B, Sq, Hq, Dh), 0, 2, 1, 3)
        k = (lambda x, n, axis: torch.repeat_interleave(x, n, dim=axis))(k_cache[:, :, Sk - W:, :], rep, axis=1)
        v = (lambda x, n, axis: torch.repeat_interleave(x, n, dim=axis))(v_cache[:, :, Sk - W:, :], rep, axis=1)
        scores = torch.einsum("bhqd,bhkd->bhqk", q, k) * (Dh ** -0.5)
        p = _softmax(scores, axis=-1)
        o = torch.einsum("bhqk,bhkd->bhqd", p, v)
        return (lambda x, *d: x.permute(*d))(o, 0, 2, 1, 3).reshape(B, Sq, Hq * Dh) @ wo

    def _B_ref(x, w1, w2):
        h = torch.einsum("bsd,df->bsf", x, w1)
        h = _silu(h)
        out = torch.einsum("bsf,fd->bsd", h, w2)
        return out

    x = A_x
    xn = _layernorm(x, axis=-1)
    return x + _A_ref(xn, A_k_cache, A_v_cache, A_wq, A_wo) + _B_ref(xn, B_w1, B_w2)
```

```cpp
#include <hip/hip_runtime.h>
#include <hip/hip_bf16.h>
#include <stdint.h>

constexpr int B = 8, D = 512, HQ = 32, HKV = 8, SK = 4096, DH = 128, W = 1024, F = 1792;
constexpr int NSPLIT = 4, KPS = W / NSPLIT;

__device__ __forceinline__ float bf2f(unsigned short b) { return __builtin_bit_cast(float, (unsigned)b << 16); }
__device__ __forceinline__ unsigned short f2bf(float f) {
  __hip_bfloat16 h = __float2bfloat16(f); return __builtin_bit_cast(unsigned short, h);
}
__device__ __forceinline__ float rbf(float f) { return bf2f(f2bf(f)); }

constexpr size_t WS_Q = 0;
constexpr size_t WS_H = WS_Q + B * 4096;
constexpr size_t WS_PO = WS_H + B * F;
constexpr size_t WS_ML = WS_PO + (size_t)B * HQ * NSPLIT * DH;

__device__ void layernorm_all(const unsigned short* x, float* xn  , int tid, int nthr) {
  int wave = tid >> 6, lane = tid & 63;
  for (int b = wave; b < B; b += nthr / 64) {
    float v[8]; float s = 0.f;
    for (int j = 0; j < 8; ++j) { v[j] = bf2f(x[b * D + lane * 8 + j]); s += v[j]; }
    for (int o = 32; o; o >>= 1) s += __shfl_xor(s, o);
    float mean = s / D;
    float ss = 0.f;
    for (int j = 0; j < 8; ++j) { float d = v[j] - mean; ss += d * d; }
    for (int o = 32; o; o >>= 1) ss += __shfl_xor(ss, o);
    float var = rbf(ss / D);
    float m = rbf(mean);
    float den = rbf(sqrtf(rbf(var + 1e-5f)));
    for (int j = 0; j < 8; ++j) xn[b * D + lane * 8 + j] = rbf(rbf(v[j] - m) / den);
  }
}

__global__ void __launch_bounds__(512) k1(const unsigned short* x, const unsigned short* wq, const unsigned short* w1, float* ws) {
  __shared__ float xn[B * D];
  __shared__ float red[8][B][64];
  int tid = threadIdx.x;
  layernorm_all(x, xn, tid, 512);
  __syncthreads();
  int blk = blockIdx.x; bool isq = blk < 64;
  const unsigned short* w = isq ? wq : w1; int ld = isq ? 4096 : F; int c0 = isq ? blk * 64 : (blk - 64) * 64;
  int wave = tid >> 6, lane = tid & 63;
  float acc[B]; for (int b = 0; b < B; ++b) acc[b] = 0.f;
  for (int k = wave * 64; k < wave * 64 + 64; ++k) {
    float wv = bf2f(w[(size_t)k * ld + c0 + lane]);
    for (int b = 0; b < B; ++b) acc[b] += xn[b * D + k] * wv;
  }
  for (int b = 0; b < B; ++b) red[wave][b][lane] = acc[b];
  __syncthreads();
  { int b = wave; float s = 0.f; for (int w8 = 0; w8 < 8; ++w8) s += red[w8][b][lane];
    s = rbf(s);
    if (isq) ws[WS_Q + b * 4096 + c0 + lane] = s;
    else { float sg = s / (1.f + __expf(-s)); ws[WS_H + b * F + c0 + lane] = rbf(sg); } }
}

__global__ void __launch_bounds__(256) k2(const unsigned short* kc, const unsigned short* vc, float* ws) {
  __shared__ float q[4][DH]; __shared__ float p[4][KPS]; __shared__ float red[4][4];
  int blk = blockIdx.x, s = blk % NSPLIT, kvh = (blk / NSPLIT) % HKV, b = blk / (NSPLIT * HKV);
  int tid = threadIdx.x, wave = tid >> 6, lane = tid & 63;
  for (int i = tid; i < 4 * DH; i += 256) q[i / DH][i % DH] = ws[WS_Q + b * 4096 + kvh * 512 + i];
  __syncthreads();
  const unsigned short* kb = kc + (((size_t)b * HKV + kvh) * SK + (SK - W) + s * KPS) * DH;
  const unsigned short* vb = vc + (((size_t)b * HKV + kvh) * SK + (SK - W) + s * KPS) * DH;
  float sc[4] = {0, 0, 0, 0};
  for (int d = 0; d < DH; d += 8) {
    uint4 u = *(const uint4*)(kb + (size_t)tid * DH + d);
    unsigned uu[4] = {u.x, u.y, u.z, u.w};
    for (int j = 0; j < 4; ++j) { float lo = bf2f(uu[j] & 0xffff), hi = bf2f(uu[j] >> 16);
      for (int g = 0; g < 4; ++g) sc[g] += q[g][d + 2 * j] * lo + q[g][d + 2 * j + 1] * hi; }
  }
  const float scale = rbf(0.08838834764831845f);
  float m[4];
  for (int g = 0; g < 4; ++g) { sc[g] = rbf(rbf(sc[g]) * 0.08838834764831845f); float t = sc[g];
    for (int o = 32; o; o >>= 1) t = fmaxf(t, __shfl_xor(t, o)); if (lane == 0) red[wave][g] = t; }
  (void)scale;
  __syncthreads();
  for (int g = 0; g < 4; ++g) m[g] = fmaxf(fmaxf(red[0][g], red[1][g]), fmaxf(red[2][g], red[3][g]));
  __syncthreads();
  float l[4];
  for (int g = 0; g < 4; ++g) { float e = __expf(sc[g] - m[g]); p[g][tid] = e; float t = e;
    for (int o = 32; o; o >>= 1) t += __shfl_xor(t, o); if (lane == 0) red[wave][g] = t; }
  __syncthreads();
  for (int g = 0; g < 4; ++g) l[g] = red[0][g] + red[1][g] + red[2][g] + red[3][g];
  int d = tid & 127, g0 = (tid >> 7) * 2; float o0 = 0, o1 = 0;
  for (int k = 0; k < KPS; ++k) { float v = bf2f(vb[(size_t)k * DH + d]); o0 += p[g0][k] * v; o1 += p[g0 + 1][k] * v; }
  size_t r0 = ((size_t)b * HQ + kvh * 4 + g0) * NSPLIT + s, r1 = r0 + NSPLIT;
  ws[WS_PO + r0 * DH + d] = o0; ws[WS_PO + r1 * DH + d] = o1;
  if (d == 0) { ws[WS_ML + r0 * 2] = m[g0]; ws[WS_ML + r0 * 2 + 1] = l[g0]; ws[WS_ML + r1 * 2] = m[g0 + 1]; ws[WS_ML + r1 * 2 + 1] = l[g0 + 1]; }
}

__global__ void __launch_bounds__(512) k3(const unsigned short* x, const unsigned short* wo, const unsigned short* w2, const float* ws, unsigned short* out) {
  __shared__ float o[4096]; __shared__ float redA[8][64], redB[8][64];
  int b = blockIdx.x / 8, c0 = (blockIdx.x % 8) * 64, tid = threadIdx.x, wave = tid >> 6, lane = tid & 63;
  for (int i = tid; i < 4096; i += 512) { int h = i / DH, d = i % DH; size_t r = ((size_t)b * HQ + h) * NSPLIT;
    float M = -INFINITY; for (int s = 0; s < NSPLIT; ++s) M = fmaxf(M, ws[WS_ML + (r + s) * 2]);
    float L = 0, a = 0; for (int s = 0; s < NSPLIT; ++s) { float wt = __expf(ws[WS_ML + (r + s) * 2] - M); L += wt * ws[WS_ML + (r + s) * 2 + 1]; a += wt * ws[WS_PO + (r + s) * DH + d]; }
    o[i] = rbf(a / L); }
  __syncthreads();
  float a = 0.f, bb = 0.f;
  for (int k = wave * 512; k < wave * 512 + 512; ++k) a += o[k] * bf2f(wo[(size_t)k * D + c0 + lane]);
  for (int k = wave * 224; k < wave * 224 + 224; ++k) bb += ws[WS_H + b * F + k] * bf2f(w2[(size_t)k * D + c0 + lane]);
  redA[wave][lane] = a; redB[wave][lane] = bb;
  __syncthreads();
  if (wave == 0) { float A = 0, Bv = 0; for (int w8 = 0; w8 < 8; ++w8) { A += redA[w8][lane]; Bv += redB[w8][lane]; }
    float xv = bf2f(x[b * D + c0 + lane]);
    out[b * D + c0 + lane] = f2bf(rbf(xv + rbf(A)) + rbf(Bv)); }
}

extern "C" void kernel_launch(void* const* d_in, const int* in_sizes, int n_in,
                              void* d_out, int out_size, void* d_ws, size_t ws_size,
                              hipStream_t stream) {
  const unsigned short* x = (const unsigned short*)d_in[0];
  const unsigned short* kc = (const unsigned short*)d_in[1];
  const unsigned short* vc = (const unsigned short*)d_in[2];
  const unsigned short* wq = (const unsigned short*)d_in[3];
  const unsigned short* wo = (const unsigned short*)d_in[4];
  const unsigned short* w1 = (const unsigned short*)d_in[6];
  const unsigned short* w2 = (const unsigned short*)d_in[7];
  float* ws = (float*)d_ws;
  k1<<<92, 512, 0, stream>>>(x, wq, w1, ws);
  k2<<<B * HKV * NSPLIT, 256, 0, stream>>>(kc, vc, ws);
  k3<<<64, 512, 0, stream>>>(x, wo, w2, ws, (unsigned short*)d_out);
}
```

```cpp
#include <hip/hip_runtime.h>
#include <stdint.h>

#define GAS __attribute__((address_space(1)))
#define LAS __attribute__((address_space(3)))
typedef unsigned u32;
typedef unsigned long long u64;
typedef u32 v4u __attribute__((ext_vector_type(4)));
typedef u32 v2u __attribute__((ext_vector_type(2)));
typedef float f32x4 __attribute__((ext_vector_type(4)));
typedef float f32x2 __attribute__((ext_vector_type(2)));
typedef short s16x8 __attribute__((ext_vector_type(8)));
typedef short s16x4 __attribute__((ext_vector_type(4)));
typedef __bf16 bf16x2 __attribute__((ext_vector_type(2)));

#define KV_NT 1
#define W_NT 1
#define WIN 2
#define GSLEEP0 16
#define GSLEEP 8
#define ROT 0
#define LPRIO 3
#define FSLEEP0 2
#define FSLEEP 1
#define HSLEEP0 2
#define HSLEEP 2

namespace {

constexpr int DM = 512, HKV = 8, SK = 4096, DH = 128, WINDOW = 1024, FF = 1792;
constexpr int NMAIN = 8, NWAVES = 10, NTHR = NWAVES * 64, NWG = 256;

constexpr u32 WS_EPOCH = 0;
constexpr u32 WS_Q = 4096;
constexpr u32 WS_H = WS_Q + 8 * 8 * 4 * 128 * 16;
constexpr u32 WS_O = WS_H + 28 * 8 * 8 * 16 * 16;
constexpr u32 WS_ML = WS_O + 32 * 8 * 4 * 32 * 16;
constexpr u32 WS_F = WS_ML + 32 * 32 * 16;
constexpr u32 WS_END = WS_F + 8 * 4 * 2 * 32 * 16 * 16;
constexpr u32 WS_ST = WS_END;

constexpr u32 L_K = 0, L_V = 65536, L_W = 131072, L_W1 = L_W + 16384;
constexpr u32 L_XQ = L_W + 24576;
constexpr u32 L_XH = L_XQ + 8 * 288;
constexpr u32 L_QI = L_XH + 8 * 160;
constexpr u32 L_TR = L_QI + 128;
constexpr u32 L_ML = L_TR + 4096;
constexpr u32 L_FLAG = L_ML + 160;
constexpr u32 L_END = L_FLAG + 192;
static_assert(L_END <= 163840, "LDS");
constexpr u32 LDS_BYTES = 163840;

constexpr u32 SPIN_CAP = 1u << 16;

struct Params {
  const char* x; const char* kc; const char* vc; const char* wq; const char* wo; const char* w1; const char* w2;
  char* out; char* ws;
};

__device__ __forceinline__ float bflo(u32 u) { return __builtin_bit_cast(float, u << 16); }
__device__ __forceinline__ float bfhi(u32 u) { return __builtin_bit_cast(float, u & 0xffff0000u); }
__device__ __forceinline__ u32 pk2(float lo, float hi) {
  f32x2 v = {lo, hi};
  return __builtin_bit_cast(u32, __builtin_convertvector(v, bf16x2));
}
__device__ __forceinline__ float rbf(float f) { return bflo(pk2(f, f) & 0xffffu); }

template <int CTRL, int RM> __device__ __forceinline__ float dppz(float v) {
  return __builtin_bit_cast(float, __builtin_amdgcn_update_dpp(0, __builtin_bit_cast(int, v), CTRL, RM, 0xf, true));
}
template <int CTRL> __device__ __forceinline__ float dppm(float v) {
  return __builtin_bit_cast(float, __builtin_amdgcn_mov_dpp(__builtin_bit_cast(int, v), CTRL, 0xf, 0xf, true));
}
__device__ __forceinline__ float wave_sum(float v) {
  v += dppz<0x111, 0xf>(v); v += dppz<0x112, 0xf>(v); v += dppz<0x114, 0xf>(v); v += dppz<0x118, 0xf>(v);
  v += dppz<0x142, 0xa>(v); v += dppz<0x143, 0xc>(v);
  return __builtin_bit_cast(float, __builtin_amdgcn_readlane(__builtin_bit_cast(int, v), 63));
}
__device__ __forceinline__ float u2f(u32 u) { return __builtin_bit_cast(float, u); }
__device__ __forceinline__ u32 f2u(float f) { return __builtin_bit_cast(u32, f); }
__device__ __forceinline__ float x16_32_max(float x) {
  auto s = __builtin_amdgcn_permlane16_swap(f2u(x), f2u(x), false, false);
  const u32 s0 = s[0], s1 = s[1];
  x = fmaxf(u2f(s0), u2f(s1));
  auto t = __builtin_amdgcn_permlane32_swap(f2u(x), f2u(x), false, false);
  const u32 t0 = t[0], t1 = t[1];
  return fmaxf(u2f(t0), u2f(t1));
}
__device__ __forceinline__ float x16_32_sum(float x) {
  auto s = __builtin_amdgcn_permlane16_swap(f2u(x), f2u(x), false, false);
  const u32 s0 = s[0], s1 = s[1];
  x = u2f(s0) + u2f(s1);
  auto t = __builtin_amdgcn_permlane32_swap(f2u(x), f2u(x), false, false);
  const u32 t0 = t[0], t1 = t[1];
  return u2f(t0) + u2f(t1);
}

struct Tag { u32 t0, t1, t2, t3; };
__device__ __forceinline__ u32 pkw(float f, u32 t) { return ((__builtin_bit_cast(u32, f) + 0x800u) & 0xfffff000u) | t; }
__device__ __forceinline__ v4u pack_g(f32x4 v, const Tag& t) { return v4u{pkw(v[0], t.t0), pkw(v[1], t.t1), pkw(v[2], t.t2), pkw(v[3], t.t3)}; }
__device__ __forceinline__ u32 bad_g(v4u g, const Tag& t) { return ((g[0] ^ t.t0) | (g[1] ^ t.t1) | (g[2] ^ t.t2) | (g[3] ^ t.t3)) & 0xfffu; }
__device__ __forceinline__ f32x4 val_g(v4u g) {
  return f32x4{__builtin_bit_cast(float, g[0] & 0xfffff000u), __builtin_bit_cast(float, g[1] & 0xfffff000u),
               __builtin_bit_cast(float, g[2] & 0xfffff000u), __builtin_bit_cast(float, g[3] & 0xfffff000u)};
}
constexpr int AUX_SC1 = 16;
__device__ __forceinline__ v4u ld_g(__amdgpu_buffer_rsrc_t rs, u32 off) { return __builtin_amdgcn_raw_buffer_load_b128(rs, off, 0, AUX_SC1); }
__device__ __forceinline__ void st_g(__amdgpu_buffer_rsrc_t rs, u32 off, v4u v) { __builtin_amdgcn_raw_buffer_store_b128(v, rs, off, 0, AUX_SC1); }

template <int NT> __device__ __forceinline__ void glds(const char* sbase, u32 voff, u32 dst) {
  if constexpr (NT)
    asm volatile("s_mov_b32 m0, %2\n\ts_nop 0\n\tglobal_load_lds_dwordx4 %0, %1 nt" ::"v"(voff), "s"(sbase), "s"(dst) : "memory");
  else
    asm volatile("s_mov_b32 m0, %2\n\ts_nop 0\n\tglobal_load_lds_dwordx4 %0, %1" ::"v"(voff), "s"(sbase), "s"(dst) : "memory");
}
template <int N> __device__ __forceinline__ void waitvm() { asm volatile("s_waitcnt vmcnt(%0)" ::"i"(N) : "memory"); }
__device__ __forceinline__ void wg_barrier() { asm volatile("s_waitcnt lgkmcnt(0)\n\ts_barrier" ::: "memory"); }

__device__ __forceinline__ f32x4 mfma(v4u a, v4u b, f32x4 c) {
  return __builtin_amdgcn_mfma_f32_16x16x32_bf16(__builtin_bit_cast(s16x8, a), __builtin_bit_cast(s16x8, b), c, 0, 0, 0);
}
__device__ __forceinline__ v2u tr_rd(LAS char* L, u32 a) {
  const s16x4 v = __builtin_amdgcn_ds_read_tr16_b64_v4i16((LAS s16x4*)(L + a));
  return __builtin_bit_cast(v2u, v);
}
__device__ __forceinline__ v4u tr_frag(LAS char* L, u32 a0, u32 a1) {
  const v2u x = tr_rd(L, a0), y = tr_rd(L, a1);
  return v4u{x[0], x[1], y[0], y[1]};
}
__device__ __forceinline__ v4u lds128(LAS char* L, u32 a) { return *(LAS v4u*)(L + a); }

#define STAMP(k) do { } while (0)
#define STAMPS_OUT() do { } while (0)

constexpr u32 C_A = L_FLAG, C_P = L_FLAG + 4, C_Q = L_FLAG + 8, C_M = L_FLAG + 12, C_W2 = L_FLAG + 16;
constexpr u32 F_K = L_FLAG + 32, F_P = L_FLAG + 64;
constexpr u32 L_TAG = L_FLAG + 96;
constexpr u32 L_MX = L_FLAG + 128;
static_assert(L_MX + 64 <= 163840 && L_FLAG % 16 == 0 && L_ML % 16 == 0, "LDS");

__device__ __forceinline__ u32 lds_ld(LAS char* L, u32 a) { return *(volatile LAS u32*)(L + a); }
__device__ __forceinline__ void lds_st(LAS char* L, u32 a, u32 v) { *(volatile LAS u32*)(L + a) = v; }
__device__ __forceinline__ void lds_arrive(LAS char* L, u32 a, int lane) {
  asm volatile("s_waitcnt lgkmcnt(0)" ::: "memory");
  if (lane == 0) __hip_atomic_fetch_add((LAS u32*)(L + a), 1u, __ATOMIC_RELAXED, __HIP_MEMORY_SCOPE_WORKGROUP);
}
__device__ __forceinline__ void lds_flag(LAS char* L, u32 a, int lane) {
  asm volatile("s_waitcnt lgkmcnt(0)" ::: "memory");
  if (lane == 0) lds_st(L, a, 1u);
}
template <int SLEEP> __device__ __forceinline__ void lds_wait(LAS char* L, u32 a, u32 n) {
  while (lds_ld(L, a) < n) { if (SLEEP) __builtin_amdgcn_s_sleep(SLEEP); }
  asm volatile("" ::: "memory");
}
__device__ __forceinline__ Tag make_tag(u64 e) {
  Tag t; t.t0 = (u32)e & 0xfffu; t.t1 = (u32)(e >> 12) & 0xfffu; t.t2 = t.t0; t.t3 = (u32)(e >> 24) & 0xfffu; return t;
}
__device__ __forceinline__ u32 chk(u32 acc, v4u g, const Tag& t) {
  acc = __builtin_amdgcn_bitop3_b32(acc, g[0], t.t0, 0xF6);
  acc = __builtin_amdgcn_bitop3_b32(acc, g[1], t.t1, 0xF6);
  acc = __builtin_amdgcn_bitop3_b32(acc, g[2], t.t2, 0xF6);
  acc = __builtin_amdgcn_bitop3_b32(acc, g[3], t.t3, 0xF6);
  return acc;
}
template <int N, int SLEEP> __device__ __forceinline__ u32 gather(__amdgpu_buffer_rsrc_t rs, const u32 (&off)[N], v4u (&g)[N], const Tag& tg) {
  u32 spin = 0;
  while (true) {
#pragma unroll
    for (int i = 0; i < N; ++i) g[i] = ld_g(rs, off[i]);
    u32 acc0 = 0, acc1 = 0;
#pragma unroll
    for (int i = 0; i < N; ++i) { if (i & 1) acc1 = chk(acc1, g[i], tg); else acc0 = chk(acc0, g[i], tg); }
    if (__all(((acc0 | acc1) & 0xfffu) == 0u) || ++spin > SPIN_CAP) break;
    if (SLEEP) __builtin_amdgcn_s_sleep(SLEEP);
    asm volatile("" ::: "memory");
  }
  return spin;
}
#define SPINS(k, e) do { (void)(e); } while (0)

__global__ void __launch_bounds__(768, 1) fused_decode_kernel(Params p) {
  __shared__ __attribute__((aligned(16))) char lds_raw[LDS_BYTES];
  LAS char* L = (LAS char*)lds_raw;
  const u32 lbase = (u32)(uintptr_t)L;
  const int tid = threadIdx.x, lane = tid & 63;
  const int wave = __builtin_amdgcn_readfirstlane(tid >> 6);
  const int wg = blockIdx.x, xx = (wg + ROT) & 7, j = wg >> 3;
  const int g = lane >> 4;
  asm volatile("" ::"s"(p.x), "s"(p.kc), "s"(p.vc), "s"(p.wq), "s"(p.wo), "s"(p.w1), "s"(p.w2), "s"(p.out), "s"(p.ws));
  STAMP(0);
  const int cs = j >> 2, ks = j & 3;
  const int hh = 4 * xx + (j >> 3), ns = j & 7;
  const int bb = j >> 2, sp = j & 3;
  const bool ffn = xx < 7;
  const u32 trq = (u32)((lane >> 2) & 3), trp = (u32)(lane & 3);
  const u32 trf = ((trq >> 1) & 1) | (((u32)g & 1) << 1);
  const u32 tr_row = ((u32)g * 8 + trq) * 128 + trp * 8;

  if (wave >= 4) {
    __builtin_amdgcn_s_setprio(LPRIO);
    const int w = wave - 4;
    const u32 wchunk = (u32)((lane & 7) ^ (((lane >> 4) & 1) << 1));
    const u32 wrow = (u32)(lane >> 3);
    {
      const char* b0 = p.wq + ((size_t)(ks * 128 + 16 * w) * 4096 + xx * 512 + cs * 64) * 2;
      const u32 vo = wrow * 8192 + wchunk * 16;
      glds<W_NT>(b0, vo, lbase + L_W + (2 * w) * 1024);
      glds<W_NT>(b0 + 8 * 8192, vo ^ 64u, lbase + L_W + (2 * w + 1) * 1024);
      if (ffn) {
        const char* b1 = p.w1 + ((size_t)(ns * 64 + 8 * w) * FF + hh * 64) * 2;
        const u32 vo1 = wrow * (FF * 2) + ((wchunk ^ ((u32)(w & 1) << 2)) * 16);
        glds<W_NT>(b1, vo1, lbase + L_W1 + w * 1024);
      }
    }
    wg_barrier();
    const size_t kvoff = ((((size_t)bb * HKV + xx) * SK + (SK - WINDOW) + sp * 256 + w * 32) * DH) * 2;
    const char* kb = p.kc + kvoff;
    const char* vb = p.vc + kvoff;
    const u32 r4 = (u32)(lane >> 4), c16 = (u32)(lane & 15);
    const u32 kvo = r4 * 256 + ((c16 ^ r4) * 16);
    const u32 vvo = r4 * 256 + ((c16 ^ (r4 << 1)) * 16);
    auto issueK = [&](int u) { const int pu = (u + j) & 7; glds<KV_NT>(kb + pu * 1024, kvo ^ (u32)((pu & 3) << 6), lbase + L_K + w * 8192 + pu * 1024); };
    auto issueV = [&](int u) { const int pu = (u + j) & 7; glds<KV_NT>(vb + pu * 1024, vvo ^ (u32)((pu & 1) << 7), lbase + L_V + w * 8192 + pu * 1024); };
    issueK(0); issueK(1); issueK(2);
    waitvm<3>();
    lds_arrive(L, C_A, lane);
    STAMP(1);
    issueK(3); waitvm<WIN>();
    issueK(4); waitvm<WIN>();
    issueK(5); waitvm<WIN>();
    issueK(6); waitvm<WIN>();
    issueK(7); waitvm<WIN>();
    issueV(0); waitvm<WIN>();
    issueV(1); waitvm<2>();
    lds_flag(L, F_K + w * 4, lane);
    STAMP(2);
    issueV(2); waitvm<WIN>();
    issueV(3); waitvm<WIN>();
    issueV(4); waitvm<WIN>();
    issueV(5); waitvm<WIN>();
    issueV(6); waitvm<WIN>();
    issueV(7); waitvm<WIN>();
    lds_wait<1>(L, C_P, 4);
    {
      const char* b0 = p.wo + ((size_t)(hh * 128 + 16 * w) * DM + ns * 64) * 2;
      const u32 vo = wrow * 1024 + wchunk * 16;
      glds<W_NT>(b0, vo, lbase + L_W + (2 * w) * 1024); waitvm<WIN>();
      glds<W_NT>(b0 + 8 * 1024, vo ^ 64u, lbase + L_W + (2 * w + 1) * 1024);
      if (ffn) {
        const char* b1 = p.w2 + ((size_t)(hh * 64 + 8 * w) * DM + ns * 64) * 2;
        glds<W_NT>(b1, wrow * 1024 + ((wchunk ^ ((u32)(w & 1) << 2)) * 16), lbase + L_W1 + w * 1024);
      }
    }
    STAMP(3);
    lds_wait<1>(L, F_P + w * 4, 1);
    const v4u P = lds128(L, L_K + w * 8192 + 4096 + g * 64 + (lane & 3) * 16);
    const u32 vrow = (u32)g * 4 + trq;
    const u32 va = L_V + w * 8192 + vrow * 256 + ((vrow & 7) * 32) + trp * 8;
    STAMP(4);
    if (ffn) waitvm<3>(); else waitvm<2>();
    STAMP(5);
    {
      v4u A[8];
#pragma unroll
      for (int dt = 0; dt < 8; ++dt) { const u32 a = va ^ (u32)(dt << 5); A[dt] = tr_frag(L, a, a + 4096); }
      f32x4 o[8];
#pragma unroll
      for (int dt = 0; dt < 8; ++dt) o[dt] = mfma(A[dt], P, f32x4{0.f, 0.f, 0.f, 0.f});
      if ((lane & 15) < 4) {
#pragma unroll
        for (int dt = 0; dt < 8; ++dt) *(LAS f32x4*)(L + L_K + w * 8192 + (lane & 3) * 512 + dt * 64 + g * 16) = o[dt];
      }
    }
    wg_barrier();
    STAMP(6);
    waitvm<0>();
    lds_arrive(L, C_W2, lane);
    if (w < 4) {
      const __amdgpu_buffer_rsrc_t rs = __builtin_amdgcn_make_buffer_rsrc((void*)p.ws, 0, (int)WS_END, 0x00020000);
      const v4u tv = lds128(L, L_TAG);
      const Tag tg{tv[0], tv[1], tv[2], tv[3]};
      lds_wait<1>(L, C_W2, 8);
      STAMP(7);
      const int nt = w;
      const u32 a0 = tr_row + (((u32)nt ^ trf) * 32);
      v4u A[4];
#pragma unroll
      for (int t = 0; t < 4; ++t) A[t] = tr_frag(L, L_W + a0 + t * 4096, L_W + a0 + t * 4096 + 512);
      f32x4 acc = {0.f, 0.f, 0.f, 0.f};
      if (ffn) {
#pragma unroll
        for (int t = 0; t < 2; ++t) {
          const v4u A2 = tr_frag(L, L_W1 + a0 + t * 4096, L_W1 + a0 + t * 4096 + 512);
          const v4u B = lds128(L, L_XH + (lane & 7) * 160 + (4 * t + g) * 16);
          acc = mfma(B, A2, acc);
        }
      }
      wg_barrier();
      STAMP(8);
#pragma unroll
      for (int t = 0; t < 4; ++t) {
        const v4u B = lds128(L, L_XQ + (lane & 7) * 288 + (4 * t + g) * 16);
        acc = mfma(B, A[t], acc);
      }
      if (lane < 32) st_g(rs, WS_F + ((((ns * 4 + nt) * 2 + g) * 32 + hh) * 16 + (lane & 15)) * 16, pack_g(acc, tg));
      STAMP(9);
    }
    STAMPS_OUT();
    __builtin_amdgcn_s_waitcnt(0);
    __builtin_amdgcn_endpgm();
    __builtin_unreachable();
  } else {
    const int w = wave;
    const __amdgpu_buffer_rsrc_t rs = __builtin_amdgcn_make_buffer_rsrc((void*)p.ws, 0, (int)WS_END, 0x00020000);
    const int fnt = hh & 3, fG = (hh >> 2) & 1, fcq = hh >> 3;
    const int fb = 4 * fG + 2 * (lane >> 5) + ((lane >> 4) & 1);
    const int fcol = ns * 64 + fnt * 16 + 4 * fcq + (lane & 3);
    v4u xv[2];
    v2u ev;
    u32 xres;
    {
      const u32 xo = (u32)(2 * w * 1024 + lane * 16), zero = 0, ro = (u32)(fb * DM + fcol) * 2;
      asm volatile("global_load_dwordx4 %0, %1, %2" : "=v"(xv[0]) : "v"(xo), "s"(p.x) : "memory");
      asm volatile("global_load_dwordx4 %0, %1, %2 offset:1024" : "=v"(xv[1]) : "v"(xo), "s"(p.x) : "memory");
      asm volatile("global_load_dwordx2 %0, %1, %2 sc1" : "=v"(ev) : "v"(zero), "s"(p.ws) : "memory");
      asm volatile("global_load_ushort %0, %1, %2" : "=v"(xres) : "v"(ro), "s"(p.x) : "memory");
    }
    if (w == 3 && lane < 32) lds_st(L, L_FLAG + lane * 4, 0u);
    wg_barrier();
    asm volatile("s_waitcnt vmcnt(2)" : "+v"(xv[0]), "+v"(xv[1])::"memory");
    STAMP(11);
    {
      float v[2][8], s1[2], s2[2], piv[2];
#pragma unroll
      for (int r = 0; r < 2; ++r) {
#pragma unroll
        for (int q = 0; q < 4; ++q) { v[r][2 * q] = bflo(xv[r][q]); v[r][2 * q + 1] = bfhi(xv[r][q]); }
        piv[r] = u2f(__builtin_amdgcn_readfirstlane(f2u(v[r][0])));
        float d[8];
#pragma unroll
        for (int q = 0; q < 8; ++q) d[q] = v[r][q] - piv[r];
        s1[r] = ((d[0] + d[1]) + (d[2] + d[3])) + ((d[4] + d[5]) + (d[6] + d[7]));
        s2[r] = fmaf(d[1], d[1], d[0] * d[0]) + fmaf(d[3], d[3], d[2] * d[2]) + (fmaf(d[5], d[5], d[4] * d[4]) + fmaf(d[7], d[7], d[6] * d[6]));
      }
#define RED4(C, RM) { const float a = dppz<C, RM>(s1[0]), b = dppz<C, RM>(s2[0]), c = dppz<C, RM>(s1[1]), d = dppz<C, RM>(s2[1]); s1[0] += a; s2[0] += b; s1[1] += c; s2[1] += d; }
      RED4(0x111, 0xf) RED4(0x112, 0xf) RED4(0x114, 0xf) RED4(0x118, 0xf) RED4(0x142, 0xa) RED4(0x143, 0xc)
#undef RED4
#pragma unroll
      for (int r = 0; r < 2; ++r) {
        const float t1 = u2f(__builtin_amdgcn_readlane(f2u(s1[r]), 63)), t2 = u2f(__builtin_amdgcn_readlane(f2u(s2[r]), 63));
        const float md = t1 * (1.f / DM);
        const float var = rbf(fmaxf(fmaf(-md, md, t2 * (1.f / DM)), 0.f));
        const float mean = rbf(piv[r] + md);
        const float den = rbf(__builtin_amdgcn_sqrtf(rbf(var + 1e-5f)));
        const float rd = __builtin_amdgcn_rcpf(den);
        v4u xn;
#pragma unroll
        for (int q = 0; q < 4; ++q) {
          const u32 d2 = pk2(v[r][2 * q] - mean, v[r][2 * q + 1] - mean);
          xn[q] = pk2(bflo(d2) * rd, bfhi(d2) * rd);
        }
        if ((lane >> 4) == ks) *(LAS v4u*)(L + L_XQ + (2 * w + r) * 288 + (lane & 15) * 16) = xn;
        if ((lane >> 3) == ns) *(LAS v4u*)(L + L_XH + (2 * w + r) * 160 + (lane & 7) * 16) = xn;
      }
    }
    asm volatile("s_waitcnt vmcnt(0)" : "+v"(ev), "+v"(xres)::"memory");
    const u64 e = (((u64)(u32)__builtin_amdgcn_readfirstlane(ev[1]) << 32) | (u32)__builtin_amdgcn_readfirstlane(ev[0])) + 1;
    const Tag tg = make_tag(e);
    if (w == 0 && lane == 0) *(LAS v4u*)(L + L_TAG) = v4u{tg.t0, tg.t1, tg.t2, tg.t3};
    lds_arrive(L, C_A, lane);
    STAMP(1);
    lds_wait<1>(L, C_A, 12);
    STAMP(2);
    {
      const u32 a0 = tr_row + (((u32)w ^ trf) * 32);
      v4u A[4], B[4], A2[2], B2[2];
#pragma unroll
      for (int t = 0; t < 4; ++t) { A[t] = tr_frag(L, L_W + a0 + t * 4096, L_W + a0 + t * 4096 + 512); B[t] = lds128(L, L_XQ + (lane & 7) * 288 + (4 * t + g) * 16); }
      if (ffn) {
#pragma unroll
        for (int t = 0; t < 2; ++t) { A2[t] = tr_frag(L, L_W1 + a0 + t * 4096, L_W1 + a0 + t * 4096 + 512); B2[t] = lds128(L, L_XH + (lane & 7) * 160 + (4 * t + g) * 16); }
      }
      f32x4 acc = {0.f, 0.f, 0.f, 0.f}, acc2 = {0.f, 0.f, 0.f, 0.f};
#pragma unroll
      for (int t = 0; t < 4; ++t) acc = mfma(A[t], B[t], acc);
      if (ffn) {
#pragma unroll
        for (int t = 0; t < 2; ++t) acc2 = mfma(A2[t], B2[t], acc2);
      }
      LAS char* T = L + L_TR + w * 1024;
      if ((lane & 15) < 8) { *(LAS f32x4*)(T + (lane & 7) * 64 + g * 16) = acc; *(LAS f32x4*)(T + 512 + (lane & 7) * 64 + g * 16) = acc2; }
      const f32x4 tq = *(LAS f32x4*)(T + lane * 16);
      const int tb = (lane >> 2) & 7, tG = lane & 3;
      const u32 oq = WS_Q + (((xx * 8 + tb) * 4 + ks) * 128 + cs * 16 + w * 4 + tG) * 16;
      const u32 oh = WS_H + (((hh * 8 + ns) * 8 + tb) * 16 + w * 4 + tG) * 16;
      if (lane < 32 || ffn) st_g(rs, lane < 32 ? oq : oh, pack_g(tq, tg));
    }
    lds_arrive(L, C_P, lane);
    STAMP(3);
    if (w < 2) {
      __builtin_amdgcn_s_sleep(GSLEEP0);
      const u32 qo = WS_Q + ((xx * 8 + bb) * 4 * 128 + w * 64 + lane) * 16;
      const u32 off[4] = {qo, qo + 2048, qo + 4096, qo + 6144};
      v4u ga[4];
      SPINS(12, (gather<4, GSLEEP>(rs, off, ga, tg)));
      const f32x4 a = (val_g(ga[0]) + val_g(ga[1])) + (val_g(ga[2]) + val_g(ga[3]));
      *(LAS v2u*)(L + L_QI + (2 * w + (lane >> 5)) * 288 + (lane & 31) * 8) = v2u{pk2(a[0], a[1]), pk2(a[2], a[3])};
      lds_arrive(L, C_Q, lane);
    } else if (ffn) {
      lds_wait<1>(L, C_P, 4);
      __builtin_amdgcn_s_sleep(GSLEEP0);
      const u32 ho = WS_H + (hh * 8 * 128 + (w - 2) * 64 + lane) * 16;
      u32 off[8];
#pragma unroll
      for (int s = 0; s < 8; ++s) off[s] = ho + s * 2048;
      v4u ga[8];
      SPINS(12, (gather<8, GSLEEP>(rs, off, ga, tg)));
      f32x4 a = ((val_g(ga[0]) + val_g(ga[1])) + (val_g(ga[2]) + val_g(ga[3]))) + ((val_g(ga[4]) + val_g(ga[5])) + (val_g(ga[6]) + val_g(ga[7])));
      float r[4];
#pragma unroll
      for (int q = 0; q < 4; ++q) { const float hv = rbf(a[q]); r[q] = hv * __builtin_amdgcn_rcpf(1.f + __builtin_amdgcn_exp2f(-hv * 1.4426950408889634f)); }
      *(LAS v2u*)(L + L_XH + (4 * (w - 2) + (lane >> 4)) * 160 + (lane & 15) * 8) = v2u{pk2(r[0], r[1]), pk2(r[2], r[3])};
    }
    STAMP(4);
    lds_wait<1>(L, C_Q, 2);
    lds_wait<1>(L, F_K + (2 * w) * 4, 1);
    lds_wait<1>(L, F_K + (2 * w + 1) * 4, 1);
    STAMP(5);
    {
      const u32 c16 = (u32)(lane & 15);
      float s[2][8], mw[2];
      v4u B[4];
#pragma unroll
      for (int t = 0; t < 4; ++t) B[t] = lds128(L, L_QI + (lane & 3) * 288 + (4 * t + g) * 16);
#pragma unroll
      for (int u = 0; u < 2; ++u) {
        f32x4 s0 = {0.f, 0.f, 0.f, 0.f}, s1 = {0.f, 0.f, 0.f, 0.f};
        const u32 ka = L_K + (2 * w + u) * 8192 + c16 * 256 + (((u32)g ^ c16) * 16);
        v4u A0[4], A1[4];
#pragma unroll
        for (int t = 0; t < 4; ++t) { A0[t] = lds128(L, ka ^ (u32)(t << 6)); A1[t] = lds128(L, (ka ^ (u32)(t << 6)) + 4096); }
#pragma unroll
        for (int t = 0; t < 4; ++t) { s0 = mfma(A0[t], B[t], s0); s1 = mfma(A1[t], B[t], s1); }
#pragma unroll
        for (int r = 0; r < 4; ++r) { s[u][r] = rbf(rbf(s0[r]) * 0.08838834764831845f); s[u][4 + r] = rbf(rbf(s1[r]) * 0.08838834764831845f); }
        mw[u] = fmaxf(fmaxf(fmaxf(s[u][0], s[u][1]), fmaxf(s[u][2], s[u][3])), fmaxf(fmaxf(s[u][4], s[u][5]), fmaxf(s[u][6], s[u][7])));
      }
      const float m = x16_32_max(fmaxf(mw[0], mw[1]));
      if (lane < 4) *(LAS float*)(L + L_MX + (lane * 4 + w) * 4) = m;
      lds_arrive(L, C_M, lane);
      lds_wait<1>(L, C_M, 4);
      const f32x4 m0 = *(LAS f32x4*)(L + L_MX + (lane & 3) * 16);
      const float M = fmaxf(fmaxf(m0[0], m0[1]), fmaxf(m0[2], m0[3]));
      float l = 0.f;
#pragma unroll
      for (int u = 0; u < 2; ++u) {
        float ex[8];
#pragma unroll
        for (int r = 0; r < 8; ++r) { ex[r] = __builtin_amdgcn_exp2f((s[u][r] - M) * 1.4426950408889634f); l += ex[r]; }
        if ((lane & 15) < 4) *(LAS v4u*)(L + L_K + (2 * w + u) * 8192 + 4096 + g * 64 + (lane & 3) * 16) = v4u{pk2(ex[0], ex[1]), pk2(ex[2], ex[3]), pk2(ex[4], ex[5]), pk2(ex[6], ex[7])};
      }
      l = x16_32_sum(l);
      if (lane < 4) { *(LAS float*)(L + L_ML + (lane * 4 + w) * 4) = l; if (w == 0) *(LAS float*)(L + L_ML + 128 + lane * 4) = M; }
      asm volatile("s_waitcnt lgkmcnt(0)" ::: "memory");
      if (lane < 2) lds_st(L, F_P + (2 * w + lane) * 4, 1u);
    }
    STAMP(6);
    wg_barrier();
    STAMP(7);
    {
      const int dq = lane & 31;
      f32x4 o[8];
#pragma unroll
      for (int u = 0; u < 8; ++u) o[u] = *(LAS f32x4*)(L + L_K + u * 8192 + w * 512 + dq * 16);
      const f32x4 l0 = *(LAS f32x4*)(L + L_ML + w * 16);
      const float M = *(LAS float*)(L + L_ML + 128 + w * 4);
      f32x4 O = ((o[0] + o[1]) + (o[2] + o[3])) + ((o[4] + o[5]) + (o[6] + o[7]));
      if (lane >= 32) O = f32x4{M, (l0[0] + l0[1]) + (l0[2] + l0[3]), 0.f, 0.f};
      const int rec = ((4 * xx + w) * 8 + bb) * 4 + sp;
      if (lane < 33) st_g(rs, lane < 32 ? WS_O + (rec * 32 + lane) * 16 : WS_ML + rec * 16, pack_g(O, tg));
    }
    STAMP(8);
    {
      const int b = lane >> 3, i = lane & 7;
      const u32 oo = WS_O + ((hh * 8 + b) * 4 * 32 + 8 * w + i) * 16;
      const u32 off[5] = {WS_ML + (hh * 32 + b * 4 + (lane & 3)) * 16, oo, oo + 512, oo + 1024, oo + 1536};
      v4u gg[5];
      __builtin_amdgcn_s_sleep(HSLEEP0);
      SPINS(13, (gather<5, HSLEEP>(rs, off, gg, tg)));
      STAMP(9);
      const float ms = u2f(gg[0][0] & 0xfffff000u), ls = u2f(gg[0][1] & 0xfffff000u);
      float M = fmaxf(ms, dppm<0xB1>(ms));
      M = fmaxf(M, dppm<0x4E>(M));
      const float ew = __builtin_amdgcn_exp2f((ms - M) * 1.4426950408889634f);
      float Ls = ew * ls;
      Ls += dppm<0xB1>(Ls);
      Ls += dppm<0x4E>(Ls);
      const float wn = ew * __builtin_amdgcn_rcpf(Ls);
      const f32x4 O = (val_g(gg[1]) * dppm<0x00>(wn) + val_g(gg[2]) * dppm<0x55>(wn)) + (val_g(gg[3]) * dppm<0xAA>(wn) + val_g(gg[4]) * dppm<0xFF>(wn));
      *(LAS v2u*)(L + L_XQ + b * 288 + (32 * w + 4 * i) * 2) = v2u{pk2(O[0], O[1]), pk2(O[2], O[3])};
      wg_barrier();
    }
    if (w == 0) {
      const u32 fo = WS_F + ((((ns * 4 + fnt) * 2 + fG) * 32 + (lane >> 2)) * 16 + 4 * fcq + (lane & 3)) * 16;
      const u32 off[2] = {fo, fo + 16 * 256};
      v4u gg[2];
      __builtin_amdgcn_s_sleep(FSLEEP0);
      SPINS(14, (gather<2, FSLEEP>(rs, off, gg, tg)));
      STAMP(10);
      const f32x4 v = val_g(gg[0]) + val_g(gg[1]);
      auto a02 = __builtin_amdgcn_permlane32_swap(f2u(v[0]), f2u(v[2]), false, false);
      auto a13 = __builtin_amdgcn_permlane32_swap(f2u(v[1]), f2u(v[3]), false, false);
      const u32 p0 = a02[0], p1 = a02[1], p2 = a13[0], p3 = a13[1];
      const float lo = u2f(p0) + u2f(p1), hi = u2f(p2) + u2f(p3);
      auto a = __builtin_amdgcn_permlane16_swap(f2u(lo), f2u(hi), false, false);
      const u32 q0 = a[0], q1 = a[1];
      float t = u2f(q0) + u2f(q1);
      t += dppm<0x124>(t);
      t += dppm<0x128>(t);
      if ((lane & 12) == 0) *(GAS unsigned short*)(p.out + (fb * DM + fcol) * 2) = (unsigned short)pk2(bflo(xres) + t, 0.f);
      if (wg == 0 && lane == 0) __hip_atomic_store((GAS u64*)(p.ws + WS_EPOCH), e, __ATOMIC_RELAXED, __HIP_MEMORY_SCOPE_AGENT);
    }
    STAMPS_OUT();
    __builtin_amdgcn_s_waitcnt(0);
    __builtin_amdgcn_endpgm();
    __builtin_unreachable();
  }
}

}

extern "C" void kernel_launch(void* const* d_in, const int* in_sizes, int n_in, void* d_out, int out_size, void* d_ws,
                              size_t ws_size, hipStream_t stream) {
  (void)in_sizes; (void)n_in; (void)out_size; (void)ws_size;
  Params p;
  p.x = (const char*)d_in[0];
  p.kc = (const char*)d_in[1];
  p.vc = (const char*)d_in[2];
  p.wq = (const char*)d_in[3];
  p.wo = (const char*)d_in[4];
  p.w1 = (const char*)d_in[6];
  p.w2 = (const char*)d_in[7];
  p.out = (char*)d_out;
  p.ws = (char*)d_ws;
  hipLaunchKernelGGL(fused_decode_kernel, dim3(NWG), dim3(768), 0, stream, p);
}
```

```cpp
#include <hip/hip_runtime.h>
#include <stdint.h>

#define GAS __attribute__((address_space(1)))
#define LAS __attribute__((address_space(3)))
typedef unsigned u32;
typedef unsigned long long u64;
typedef u32 v4u __attribute__((ext_vector_type(4)));
typedef u32 v2u __attribute__((ext_vector_type(2)));
typedef float f32x4 __attribute__((ext_vector_type(4)));
typedef float f32x2 __attribute__((ext_vector_type(2)));
typedef short s16x8 __attribute__((ext_vector_type(8)));
typedef short s16x4 __attribute__((ext_vector_type(4)));
typedef __bf16 bf16x2 __attribute__((ext_vector_type(2)));

#define KV_NT 1
#define W_NT 1
#define WIN 2
#define GSLEEP0 16
#define GSLEEP 8
#define ROT 0
#define LPRIO 3
#define FSLEEP0 2
#define FSLEEP 1
#define HSLEEP0 2
#define HSLEEP 2

namespace {

constexpr int DM = 512, HKV = 8, SK = 4096, DH = 128, WINDOW = 1024, FF = 1792;
constexpr int NMAIN = 8, NWAVES = 10, NTHR = NWAVES * 64, NWG = 256;

constexpr u32 WS_EPOCH = 0;
constexpr u32 WS_Q = 4096;
constexpr u32 WS_H = WS_Q + 8 * 8 * 4 * 128 * 16;
constexpr u32 WS_O = WS_H + 28 * 8 * 8 * 16 * 16;
constexpr u32 WS_ML = WS_O + 32 * 8 * 4 * 32 * 16;
constexpr u32 WS_F = WS_ML + 32 * 32 * 16;
constexpr u32 WS_END = WS_F + 8 * 4 * 2 * 32 * 16 * 16;
constexpr u32 WS_ST = WS_END;

constexpr u32 L_K = 0, L_V = 65536, L_W = 131072, L_W1 = L_W + 16384;
constexpr u32 L_XQ = L_W + 24576;
constexpr u32 L_XH = L_XQ + 8 * 288;
constexpr u32 L_QI = L_XH + 8 * 160;
constexpr u32 L_TR = L_QI + 128;
constexpr u32 L_ML = L_TR + 4096;
constexpr u32 L_FLAG = L_ML + 160;
constexpr u32 L_END = L_FLAG + 192;
static_assert(L_END <= 163840, "LDS");
constexpr u32 LDS_BYTES = 163840;

constexpr u32 SPIN_CAP = 1u << 16;

struct Params {
  const char* x; const char* kc; const char* vc; const char* wq; const char* wo; const char* w1; const char* w2;
  char* out; char* ws;
};

__device__ __forceinline__ float bflo(u32 u) { return __builtin_bit_cast(float, u << 16); }
__device__ __forceinline__ float bfhi(u32 u) { return __builtin_bit_cast(float, u & 0xffff0000u); }
__device__ __forceinline__ u32 pk2(float lo, float hi) {
  f32x2 v = {lo, hi};
  return __builtin_bit_cast(u32, __builtin_convertvector(v, bf16x2));
}
__device__ __forceinline__ float rbf(float f) { return bflo(pk2(f, f) & 0xffffu); }

template <int CTRL, int RM> __device__ __forceinline__ float dppz(float v) {
  return __builtin_bit_cast(float, __builtin_amdgcn_update_dpp(0, __builtin_bit_cast(int, v), CTRL, RM, 0xf, true));
}
template <int CTRL> __device__ __forceinline__ float dppm(float v) {
  return __builtin_bit_cast(float, __builtin_amdgcn_mov_dpp(__builtin_bit_cast(int, v), CTRL, 0xf, 0xf, true));
}
__device__ __forceinline__ float wave_sum(float v) {
  v += dppz<0x111, 0xf>(v); v += dppz<0x112, 0xf>(v); v += dppz<0x114, 0xf>(v); v += dppz<0x118, 0xf>(v);
  v += dppz<0x142, 0xa>(v); v += dppz<0x143, 0xc>(v);
  return __builtin_bit_cast(float, __builtin_amdgcn_readlane(__builtin_bit_cast(int, v), 63));
}
__device__ __forceinline__ float u2f(u32 u) { return __builtin_bit_cast(float, u); }
__device__ __forceinline__ u32 f2u(float f) { return __builtin_bit_cast(u32, f); }
__device__ __forceinline__ float x16_32_max(float x) {
  auto s = __builtin_amdgcn_permlane16_swap(f2u(x), f2u(x), false, false);
  const u32 s0 = s[0], s1 = s[1];
  x = fmaxf(u2f(s0), u2f(s1));
  auto t = __builtin_amdgcn_permlane32_swap(f2u(x), f2u(x), false, false);
  const u32 t0 = t[0], t1 = t[1];
  return fmaxf(u2f(t0), u2f(t1));
}
__device__ __forceinline__ float x16_32_sum(float x) {
  auto s = __builtin_amdgcn_permlane16_swap(f2u(x), f2u(x), false, false);
  const u32 s0 = s[0], s1 = s[1];
  x = u2f(s0) + u2f(s1);
  auto t = __builtin_amdgcn_permlane32_swap(f2u(x), f2u(x), false, false);
  const u32 t0 = t[0], t1 = t[1];
  return u2f(t0) + u2f(t1);
}

struct Tag { u32 t0, t1, t2, t3; };
__device__ __forceinline__ u32 pkw(float f, u32 t) { return ((__builtin_bit_cast(u32, f) + 0x800u) & 0xfffff000u) | t; }
__device__ __forceinline__ v4u pack_g(f32x4 v, const Tag& t) { return v4u{pkw(v[0], t.t0), pkw(v[1], t.t1), pkw(v[2], t.t2), pkw(v[3], t.t3)}; }
__device__ __forceinline__ u32 bad_g(v4u g, const Tag& t) { return ((g[0] ^ t.t0) | (g[1] ^ t.t1) | (g[2] ^ t.t2) | (g[3] ^ t.t3)) & 0xfffu; }
__device__ __forceinline__ f32x4 val_g(v4u g) {
  return f32x4{__builtin_bit_cast(float, g[0] & 0xfffff000u), __builtin_bit_cast(float, g[1] & 0xfffff000u),
               __builtin_bit_cast(float, g[2] & 0xfffff000u), __builtin_bit_cast(float, g[3] & 0xfffff000u)};
}
constexpr int AUX_SC1 = 16;
__device__ __forceinline__ v4u ld_g(__amdgpu_buffer_rsrc_t rs, u32 off) { return __builtin_amdgcn_raw_buffer_load_b128(rs, off, 0, AUX_SC1); }
__device__ __forceinline__ void st_g(__amdgpu_buffer_rsrc_t rs, u32 off, v4u v) { __builtin_amdgcn_raw_buffer_store_b128(v, rs, off, 0, AUX_SC1); }

template <int NT> __device__ __forceinline__ void glds(const char* sbase, u32 voff, u32 dst) {
  if constexpr (NT)
    asm volatile("s_mov_b32 m0, %2\n\ts_nop 0\n\tglobal_load_lds_dwordx4 %0, %1 nt" ::"v"(voff), "s"(sbase), "s"(dst) : "memory");
  else
    asm volatile("s_mov_b32 m0, %2\n\ts_nop 0\n\tglobal_load_lds_dwordx4 %0, %1" ::"v"(voff), "s"(sbase), "s"(dst) : "memory");
}
template <int N> __device__ __forceinline__ void waitvm() { asm volatile("s_waitcnt vmcnt(%0)" ::"i"(N) : "memory"); }
__device__ __forceinline__ void wg_barrier() { asm volatile("s_waitcnt lgkmcnt(0)\n\ts_barrier" ::: "memory"); }

__device__ __forceinline__ f32x4 mfma(v4u a, v4u b, f32x4 c) {
  return __builtin_amdgcn_mfma_f32_16x16x32_bf16(__builtin_bit_cast(s16x8, a), __builtin_bit_cast(s16x8, b), c, 0, 0, 0);
}
__device__ __forceinline__ v2u tr_rd(LAS char* L, u32 a) {
  const s16x4 v = __builtin_amdgcn_ds_read_tr16_b64_v4i16((LAS s16x4*)(L + a));
  return __builtin_bit_cast(v2u, v);
}
__device__ __forceinline__ v4u tr_frag(LAS char* L, u32 a0, u32 a1) {
  const v2u x = tr_rd(L, a0), y = tr_rd(L, a1);
  return v4u{x[0], x[1], y[0], y[1]};
}
__device__ __forceinline__ v4u lds128(LAS char* L, u32 a) { return *(LAS v4u*)(L + a); }

#define STAMP(k) do { } while (0)
#define STAMPS_OUT() do { } while (0)

constexpr u32 C_A = L_FLAG, C_P = L_FLAG + 4, C_Q = L_FLAG + 8, C_M = L_FLAG + 12, C_W2 = L_FLAG + 16;
constexpr u32 F_K = L_FLAG + 32, F_P = L_FLAG + 64;
constexpr u32 L_TAG = L_FLAG + 96;
constexpr u32 L_MX = L_FLAG + 128;
static_assert(L_MX + 64 <= 163840 && L_FLAG % 16 == 0 && L_ML % 16 == 0, "LDS");

__device__ __forceinline__ u32 lds_ld(LAS char* L, u32 a) { return *(volatile LAS u32*)(L + a); }
__device__ __forceinline__ void lds_st(LAS char* L, u32 a, u32 v) { *(volatile LAS u32*)(L + a) = v; }
__device__ __forceinline__ void lds_arrive(LAS char* L, u32 a, int lane) {
  asm volatile("s_waitcnt lgkmcnt(0)" ::: "memory");
  if (lane == 0) __hip_atomic_fetch_add((LAS u32*)(L + a), 1u, __ATOMIC_RELAXED, __HIP_MEMORY_SCOPE_WORKGROUP);
}
__device__ __forceinline__ void lds_flag(LAS char* L, u32 a, int lane) {
  asm volatile("s_waitcnt lgkmcnt(0)" ::: "memory");
  if (lane == 0) lds_st(L, a, 1u);
}
template <int SLEEP> __device__ __forceinline__ void lds_wait(LAS char* L, u32 a, u32 n) {
  while (lds_ld(L, a) < n) { if (SLEEP) __builtin_amdgcn_s_sleep(SLEEP); }
  asm volatile("" ::: "memory");
}
__device__ __forceinline__ Tag make_tag(u64 e) {
  Tag t; t.t0 = (u32)e & 0xfffu; t.t1 = (u32)(e >> 12) & 0xfffu; t.t2 = t.t0; t.t3 = (u32)(e >> 24) & 0xfffu; return t;
}
__device__ __forceinline__ u32 chk(u32 acc, v4u g, const Tag& t) {
  acc = __builtin_amdgcn_bitop3_b32(acc, g[0], t.t0, 0xF6);
  acc = __builtin_amdgcn_bitop3_b32(acc, g[1], t.t1, 0xF6);
  acc = __builtin_amdgcn_bitop3_b32(acc, g[2], t.t2, 0xF6);
  acc = __builtin_amdgcn_bitop3_b32(acc, g[3], t.t3, 0xF6);
  return acc;
}
template <int N, int SLEEP> __device__ __forceinline__ u32 gather(__amdgpu_buffer_rsrc_t rs, const u32 (&off)[N], v4u (&g)[N], const Tag& tg) {
  u32 spin = 0;
  while (true) {
#pragma unroll
    for (int i = 0; i < N; ++i) g[i] = ld_g(rs, off[i]);
    u32 acc0 = 0, acc1 = 0;
#pragma unroll
    for (int i = 0; i < N; ++i) { if (i & 1) acc1 = chk(acc1, g[i], tg); else acc0 = chk(acc0, g[i], tg); }
    if (__all(((acc0 | acc1) & 0xfffu) == 0u) || ++spin > SPIN_CAP) break;
    if (SLEEP) __builtin_amdgcn_s_sleep(SLEEP);
    asm volatile("" ::: "memory");
  }
  return spin;
}
#define SPINS(k, e) do { (void)(e); } while (0)

__global__ void __launch_bounds__(768, 1) fused_decode_kernel(Params p) {
  __shared__ __attribute__((aligned(16))) char lds_raw[LDS_BYTES];
  LAS char* L = (LAS char*)lds_raw;
  const u32 lbase = (u32)(uintptr_t)L;
  const int tid = threadIdx.x, lane = tid & 63;
  const int wave = __builtin_amdgcn_readfirstlane(tid >> 6);
  const int wg = blockIdx.x, xx = (wg + ROT) & 7, j = wg >> 3;
  const int g = lane >> 4;
  asm volatile("" ::"s"(p.x), "s"(p.kc), "s"(p.vc), "s"(p.wq), "s"(p.wo), "s"(p.w1), "s"(p.w2), "s"(p.out), "s"(p.ws));
  STAMP(0);
  const int cs = j >> 2, ks = j & 3;
  const int hh = 4 * xx + (j >> 3), ns = j & 7;
  const int bb = j >> 2, sp = j & 3;
  const bool ffn = xx < 7;
  const u32 trq = (u32)((lane >> 2) & 3), trp = (u32)(lane & 3);
  const u32 trf = ((trq >> 1) & 1) | (((u32)g & 1) << 1);
  const u32 tr_row = ((u32)g * 8 + trq) * 128 + trp * 8;

  if (wave >= 4) {
    __builtin_amdgcn_s_setprio(LPRIO);
    const int w = wave - 4;
    const u32 wchunk = (u32)((lane & 7) ^ (((lane >> 4) & 1) << 1));
    const u32 wrow = (u32)(lane >> 3);
    {
      const char* b0 = p.wq + ((size_t)(ks * 128 + 16 * w) * 4096 + xx * 512 + cs * 64) * 2;
      const u32 vo = wrow * 8192 + wchunk * 16;
      glds<W_NT>(b0, vo, lbase + L_W + (2 * w) * 1024);
      glds<W_NT>(b0 + 8 * 8192, vo ^ 64u, lbase + L_W + (2 * w + 1) * 1024);
      if (ffn) {
        const char* b1 = p.w1 + ((size_t)(ns * 64 + 8 * w) * FF + hh * 64) * 2;
        const u32 vo1 = wrow * (FF * 2) + ((wchunk ^ ((u32)(w & 1) << 2)) * 16);
        glds<W_NT>(b1, vo1, lbase + L_W1 + w * 1024);
      }
    }
    wg_barrier();
    const size_t kvoff = ((((size_t)bb * HKV + xx) * SK + (SK - WINDOW) + sp * 256 + w * 32) * DH) * 2;
    const char* kb = p.kc + kvoff;
    const char* vb = p.vc + kvoff;
    const u32 r4 = (u32)(lane >> 4), c16 = (u32)(lane & 15);
    const u32 kvo = r4 * 256 + ((c16 ^ r4) * 16);
    const u32 vvo = r4 * 256 + ((c16 ^ (r4 << 1)) * 16);
    auto issueK = [&](int u) { const int pu = (u + j) & 7; glds<KV_NT>(kb + pu * 1024, kvo ^ (u32)((pu & 3) << 6), lbase + L_K + w * 8192 + pu * 1024); };
    auto issueV = [&](int u) { const int pu = (u + j) & 7; glds<KV_NT>(vb + pu * 1024, vvo ^ (u32)((pu & 1) << 7), lbase + L_V + w * 8192 + pu * 1024); };
    issueK(0); issueK(1); issueK(2);
    waitvm<3>();
    lds_arrive(L, C_A, lane);
    STAMP(1);
    issueK(3); waitvm<WIN>();
    issueK(4); waitvm<WIN>();
    issueK(5); waitvm<WIN>();
    issueK(6); waitvm<WIN>();
    issueK(7); waitvm<WIN>();
    issueV(0); waitvm<WIN>();
    issueV(1); waitvm<2>();
    lds_flag(L, F_K + w * 4, lane);
    STAMP(2);
    issueV(2); waitvm<WIN>();
    issueV(3); waitvm<WIN>();
    issueV(4); waitvm<WIN>();
    issueV(5); waitvm<WIN>();
    issueV(6); waitvm<WIN>();
    issueV(7); waitvm<WIN>();
    lds_wait<1>(L, C_P, 4);
    {
      const char* b0 = p.wo + ((size_t)(hh * 128 + 16 * w) * DM + ns * 64) * 2;
      const u32 vo = wrow * 1024 + wchunk * 16;
      glds<W_NT>(b0, vo, lbase + L_W + (2 * w) * 1024); waitvm<WIN>();
      glds<W_NT>(b0 + 8 * 1024, vo ^ 64u, lbase + L_W + (2 * w + 1) * 1024);
      if (ffn) {
        const char* b1 = p.w2 + ((size_t)(hh * 64 + 8 * w) * DM + ns * 64) * 2;
        glds<W_NT>(b1, wrow * 1024 + ((wchunk ^ ((u32)(w & 1) << 2)) * 16), lbase + L_W1 + w * 1024);
      }
    }
    STAMP(3);
    lds_wait<1>(L, F_P + w * 4, 1);
    const v4u P = lds128(L, L_K + w * 8192 + 4096 + g * 64 + (lane & 3) * 16);
    const u32 vrow = (u32)g * 4 + trq;
    const u32 va = L_V + w * 8192 + vrow * 256 + ((vrow & 7) * 32) + trp * 8;
    STAMP(4);
    if (ffn) waitvm<3>(); else waitvm<2>();
    STAMP(5);
    {
      v4u A[8];
#pragma unroll
      for (int dt = 0; dt < 8; ++dt) { const u32 a = va ^ (u32)(dt << 5); A[dt] = tr_frag(L, a, a + 4096); }
      f32x4 o[8];
#pragma unroll
      for (int dt = 0; dt < 8; ++dt) o[dt] = mfma(A[dt], P, f32x4{0.f, 0.f, 0.f, 0.f});
      if ((lane & 15) < 4) {
#pragma unroll
        for (int dt = 0; dt < 8; ++dt) *(LAS f32x4*)(L + L_K + w * 8192 + (lane & 3) * 512 + dt * 64 + g * 16) = o[dt];
      }
    }
    wg_barrier();
    STAMP(6);
    waitvm<0>();
    lds_arrive(L, C_W2, lane);
    if (w < 4) {
      const __amdgpu_buffer_rsrc_t rs = __builtin_amdgcn_make_buffer_rsrc((void*)p.ws, 0, (int)WS_END, 0x00020000);
      const v4u tv = lds128(L, L_TAG);
      const Tag tg{tv[0], tv[1], tv[2], tv[3]};
      lds_wait<1>(L, C_W2, 8);
      STAMP(7);
      const int nt = w;
      const u32 a0 = tr_row + (((u32)nt ^ trf) * 32);
      v4u A[4];
#pragma unroll
      for (int t = 0; t < 4; ++t) A[t] = tr_frag(L, L_W + a0 + t * 4096, L_W + a0 + t * 4096 + 512);
      f32x4 acc = {0.f, 0.f, 0.f, 0.f};
      if (ffn) {
#pragma unroll
        for (int t = 0; t < 2; ++t) {
          const v4u A2 = tr_frag(L, L_W1 + a0 + t * 4096, L_W1 + a0 + t * 4096 + 512);
          const v4u B = lds128(L, L_XH + (lane & 7) * 160 + (4 * t + g) * 16);
          acc = mfma(B, A2, acc);
        }
      }
      wg_barrier();
      STAMP(8);
#pragma unroll
      for (int t = 0; t < 4; ++t) {
        const v4u B = lds128(L, L_XQ + (lane & 7) * 288 + (4 * t + g) * 16);
        acc = mfma(B, A[t], acc);
      }
      if (lane < 32) st_g(rs, WS_F + ((((ns * 4 + nt) * 2 + g) * 32 + hh) * 16 + (lane & 15)) * 16, pack_g(acc, tg));
      STAMP(9);
    }
    STAMPS_OUT();
    __builtin_amdgcn_s_waitcnt(0);
    __builtin_amdgcn_endpgm();
    __builtin_unreachable();
  } else {
    const int w = wave;
    const __amdgpu_buffer_rsrc_t rs = __builtin_amdgcn_make_buffer_rsrc((void*)p.ws, 0, (int)WS_END, 0x00020000);
    const int fnt = hh & 3, fG = (hh >> 2) & 1, fcq = hh >> 3;
    const int fb = 4 * fG + 2 * (lane >> 5) + ((lane >> 4) & 1);
    const int fcol = ns * 64 + fnt * 16 + 4 * fcq + (lane & 3);
    v4u xv[2];
    v2u ev;
    u32 xres;
    {
      const u32 xo = (u32)(2 * w * 1024 + lane * 16), zero = 0, ro = (u32)(fb * DM + fcol) * 2;
      asm volatile("global_load_dwordx4 %0, %1, %2" : "=v"(xv[0]) : "v"(xo), "s"(p.x) : "memory");
      asm volatile("global_load_dwordx4 %0, %1, %2 offset:1024" : "=v"(xv[1]) : "v"(xo), "s"(p.x) : "memory");
      asm volatile("global_load_dwordx2 %0, %1, %2 sc1" : "=v"(ev) : "v"(zero), "s"(p.ws) : "memory");
      asm volatile("global_load_ushort %0, %1, %2" : "=v"(xres) : "v"(ro), "s"(p.x) : "memory");
    }
    if (w == 3 && lane < 32) lds_st(L, L_FLAG + lane * 4, 0u);
    u32 dump = 0;
    {
      const size_t kvo_ = ((((size_t)bb * HKV + xx) * SK + (SK - WINDOW) + sp * 256) * DH) * 2;
      const char* a;
      if (lane < 2) a = p.vc + kvo_ + lane * 65472;
      else if (lane < 5) a = p.wo + ((size_t)(hh * 128) * DM + ns * 64) * 2 + (lane - 2) * 65024;
      else if (lane < 7) a = p.w2 + ((size_t)((ffn ? hh : 0) * 64) * DM + ns * 64) * 2 + (lane - 5) * 64512;
      else if (lane < 9) a = p.kc + kvo_ + (lane - 7) * 65472;
      else if (lane == 9) a = p.out;
      else if (lane < 44) { u32 o = (u32)(lane - 10) * 65536u; if (o > WS_END - 64) o = WS_END - 64; a = p.ws + o; }
      else a = p.x;
      if (w != 3) a = p.x;
      asm volatile("global_load_dword %0, %1, off sc1" : "=&v"(dump) : "v"(a) : "memory");
    }
    wg_barrier();
    asm volatile("s_waitcnt vmcnt(3)" : "+v"(xv[0]), "+v"(xv[1])::"memory");
    STAMP(11);
    {
      float v[2][8], s1[2], s2[2], piv[2];
#pragma unroll
      for (int r = 0; r < 2; ++r) {
#pragma unroll
        for (int q = 0; q < 4; ++q) { v[r][2 * q] = bflo(xv[r][q]); v[r][2 * q + 1] = bfhi(xv[r][q]); }
        piv[r] = u2f(__builtin_amdgcn_readfirstlane(f2u(v[r][0])));
        float d[8];
#pragma unroll
        for (int q = 0; q < 8; ++q) d[q] = v[r][q] - piv[r];
        s1[r] = ((d[0] + d[1]) + (d[2] + d[3])) + ((d[4] + d[5]) + (d[6] + d[7]));
        s2[r] = fmaf(d[1], d[1], d[0] * d[0]) + fmaf(d[3], d[3], d[2] * d[2]) + (fmaf(d[5], d[5], d[4] * d[4]) + fmaf(d[7], d[7], d[6] * d[6]));
      }
#define RED4(C, RM) { const float a = dppz<C, RM>(s1[0]), b = dppz<C, RM>(s2[0]), c = dppz<C, RM>(s1[1]), d = dppz<C, RM>(s2[1]); s1[0] += a; s2[0] += b; s1[1] += c; s2[1] += d; }
      RED4(0x111, 0xf) RED4(0x112, 0xf) RED4(0x114, 0xf) RED4(0x118, 0xf) RED4(0x142, 0xa) RED4(0x143, 0xc)
#undef RED4
#pragma unroll
      for (int r = 0; r < 2; ++r) {
        const float t1 = u2f(__builtin_amdgcn_readlane(f2u(s1[r]), 63)), t2 = u2f(__builtin_amdgcn_readlane(f2u(s2[r]), 63));
        const float md = t1 * (1.f / DM);
        const float var = rbf(fmaxf(fmaf(-md, md, t2 * (1.f / DM)), 0.f));
        const float mean = rbf(piv[r] + md);
        const float den = rbf(__builtin_amdgcn_sqrtf(rbf(var + 1e-5f)));
        const float rd = __builtin_amdgcn_rcpf(den);
        v4u xn;
#pragma unroll
        for (int q = 0; q < 4; ++q) {
          const u32 d2 = pk2(v[r][2 * q] - mean, v[r][2 * q + 1] - mean);
          xn[q] = pk2(bflo(d2) * rd, bfhi(d2) * rd);
        }
        if ((lane >> 4) == ks) *(LAS v4u*)(L + L_XQ + (2 * w + r) * 288 + (lane & 15) * 16) = xn;
        if ((lane >> 3) == ns) *(LAS v4u*)(L + L_XH + (2 * w + r) * 160 + (lane & 7) * 16) = xn;
      }
    }
    asm volatile("s_waitcnt vmcnt(0)" : "+v"(ev), "+v"(xres), "+v"(dump)::"memory");
    const u64 e = (((u64)(u32)__builtin_amdgcn_readfirstlane(ev[1]) << 32) | (u32)__builtin_amdgcn_readfirstlane(ev[0])) + 1;
    const Tag tg = make_tag(e);
    if (w == 0 && lane == 0) *(LAS v4u*)(L + L_TAG) = v4u{tg.t0, tg.t1, tg.t2, tg.t3};
    lds_arrive(L, C_A, lane);
    STAMP(1);
    lds_wait<1>(L, C_A, 12);
    STAMP(2);
    {
      const u32 a0 = tr_row + (((u32)w ^ trf) * 32);
      v4u A[4], B[4], A2[2], B2[2];
#pragma unroll
      for (int t = 0; t < 4; ++t) { A[t] = tr_frag(L, L_W + a0 + t * 4096, L_W + a0 + t * 4096 + 512); B[t] = lds128(L, L_XQ + (lane & 7) * 288 + (4 * t + g) * 16); }
      if (ffn) {
#pragma unroll
        for (int t = 0; t < 2; ++t) { A2[t] = tr_frag(L, L_W1 + a0 + t * 4096, L_W1 + a0 + t * 4096 + 512); B2[t] = lds128(L, L_XH + (lane & 7) * 160 + (4 * t + g) * 16); }
      }
      f32x4 acc = {0.f, 0.f, 0.f, 0.f}, acc2 = {0.f, 0.f, 0.f, 0.f};
#pragma unroll
      for (int t = 0; t < 4; ++t) acc = mfma(A[t], B[t], acc);
      if (ffn) {
#pragma unroll
        for (int t = 0; t < 2; ++t) acc2 = mfma(A2[t], B2[t], acc2);
      }
      LAS char* T = L + L_TR + w * 1024;
      if ((lane & 15) < 8) { *(LAS f32x4*)(T + (lane & 7) * 64 + g * 16) = acc; *(LAS f32x4*)(T + 512 + (lane & 7) * 64 + g * 16) = acc2; }
      const f32x4 tq = *(LAS f32x4*)(T + lane * 16);
      const int tb = (lane >> 2) & 7, tG = lane & 3;
      const u32 oq = WS_Q + (((xx * 8 + tb) * 4 + ks) * 128 + cs * 16 + w * 4 + tG) * 16;
      const u32 oh = WS_H + (((hh * 8 + ns) * 8 + tb) * 16 + w * 4 + tG) * 16;
      if (lane < 32 || ffn) st_g(rs, lane < 32 ? oq : oh, pack_g(tq, tg));
    }
    lds_arrive(L, C_P, lane);
    STAMP(3);
    if (w < 2) {
      __builtin_amdgcn_s_sleep(GSLEEP0);
      const u32 qo = WS_Q + ((xx * 8 + bb) * 4 * 128 + w * 64 + lane) * 16;
      const u32 off[4] = {qo, qo + 2048, qo + 4096, qo + 6144};
      v4u ga[4];
      SPINS(12, (gather<4, GSLEEP>(rs, off, ga, tg)));
      const f32x4 a = (val_g(ga[0]) + val_g(ga[1])) + (val_g(ga[2]) + val_g(ga[3]));
      *(LAS v2u*)(L + L_QI + (2 * w + (lane >> 5)) * 288 + (lane & 31) * 8) = v2u{pk2(a[0], a[1]), pk2(a[2], a[3])};
      lds_arrive(L, C_Q, lane);
    } else if (ffn) {
      lds_wait<1>(L, C_P, 4);
      __builtin_amdgcn_s_sleep(GSLEEP0);
      const u32 ho = WS_H + (hh * 8 * 128 + (w - 2) * 64 + lane) * 16;
      u32 off[8];
#pragma unroll
      for (int s = 0; s < 8; ++s) off[s] = ho + s * 2048;
      v4u ga[8];
      SPINS(12, (gather<8, GSLEEP>(rs, off, ga, tg)));
      f32x4 a = ((val_g(ga[0]) + val_g(ga[1])) + (val_g(ga[2]) + val_g(ga[3]))) + ((val_g(ga[4]) + val_g(ga[5])) + (val_g(ga[6]) + val_g(ga[7])));
      float r[4];
#pragma unroll
      for (int q = 0; q < 4; ++q) { const float hv = rbf(a[q]); r[q] = hv * __builtin_amdgcn_rcpf(1.f + __builtin_amdgcn_exp2f(-hv * 1.4426950408889634f)); }
      *(LAS v2u*)(L + L_XH + (4 * (w - 2) + (lane >> 4)) * 160 + (lane & 15) * 8) = v2u{pk2(r[0], r[1]), pk2(r[2], r[3])};
    }
    STAMP(4);
    lds_wait<1>(L, C_Q, 2);
    lds_wait<1>(L, F_K + (2 * w) * 4, 1);
    lds_wait<1>(L, F_K + (2 * w + 1) * 4, 1);
    STAMP(5);
    {
      const u32 c16 = (u32)(lane & 15);
      float s[2][8], mw[2];
      v4u B[4];
#pragma unroll
      for (int t = 0; t < 4; ++t) B[t] = lds128(L, L_QI + (lane & 3) * 288 + (4 * t + g) * 16);
#pragma unroll
      for (int u = 0; u < 2; ++u) {
        f32x4 s0 = {0.f, 0.f, 0.f, 0.f}, s1 = {0.f, 0.f, 0.f, 0.f};
        const u32 ka = L_K + (2 * w + u) * 8192 + c16 * 256 + (((u32)g ^ c16) * 16);
        v4u A0[4], A1[4];
#pragma unroll
        for (int t = 0; t < 4; ++t) { A0[t] = lds128(L, ka ^ (u32)(t << 6)); A1[t] = lds128(L, (ka ^ (u32)(t << 6)) + 4096); }
#pragma unroll
        for (int t = 0; t < 4; ++t) { s0 = mfma(A0[t], B[t], s0); s1 = mfma(A1[t], B[t], s1); }
#pragma unroll
        for (int r = 0; r < 4; ++r) { s[u][r] = rbf(rbf(s0[r]) * 0.08838834764831845f); s[u][4 + r] = rbf(rbf(s1[r]) * 0.08838834764831845f); }
        mw[u] = fmaxf(fmaxf(fmaxf(s[u][0], s[u][1]), fmaxf(s[u][2], s[u][3])), fmaxf(fmaxf(s[u][4], s[u][5]), fmaxf(s[u][6], s[u][7])));
      }
      const float m = x16_32_max(fmaxf(mw[0], mw[1]));
      if (lane < 4) *(LAS float*)(L + L_MX + (lane * 4 + w) * 4) = m;
      lds_arrive(L, C_M, lane);
      lds_wait<1>(L, C_M, 4);
      const f32x4 m0 = *(LAS f32x4*)(L + L_MX + (lane & 3) * 16);
      const float M = fmaxf(fmaxf(m0[0], m0[1]), fmaxf(m0[2], m0[3]));
      float l = 0.f;
#pragma unroll
      for (int u = 0; u < 2; ++u) {
        float ex[8];
#pragma unroll
        for (int r = 0; r < 8; ++r) { ex[r] = __builtin_amdgcn_exp2f((s[u][r] - M) * 1.4426950408889634f); l += ex[r]; }
        if ((lane & 15) < 4) *(LAS v4u*)(L + L_K + (2 * w + u) * 8192 + 4096 + g * 64 + (lane & 3) * 16) = v4u{pk2(ex[0], ex[1]), pk2(ex[2], ex[3]), pk2(ex[4], ex[5]), pk2(ex[6], ex[7])};
      }
      l = x16_32_sum(l);
      if (lane < 4) { *(LAS float*)(L + L_ML + (lane * 4 + w) * 4) = l; if (w == 0) *(LAS float*)(L + L_ML + 128 + lane * 4) = M; }
      asm volatile("s_waitcnt lgkmcnt(0)" ::: "memory");
      if (lane < 2) lds_st(L, F_P + (2 * w + lane) * 4, 1u);
    }
    STAMP(6);
    wg_barrier();
    STAMP(7);
    {
      const int dq = lane & 31;
      f32x4 o[8];
#pragma unroll
      for (int u = 0; u < 8; ++u) o[u] = *(LAS f32x4*)(L + L_K + u * 8192 + w * 512 + dq * 16);
      const f32x4 l0 = *(LAS f32x4*)(L + L_ML + w * 16);
      const float M = *(LAS float*)(L + L_ML + 128 + w * 4);
      f32x4 O = ((o[0] + o[1]) + (o[2] + o[3])) + ((o[4] + o[5]) + (o[6] + o[7]));
      if (lane >= 32) O = f32x4{M, (l0[0] + l0[1]) + (l0[2] + l0[3]), 0.f, 0.f};
      const int rec = ((4 * xx + w) * 8 + bb) * 4 + sp;
      if (lane < 33) st_g(rs, lane < 32 ? WS_O + (rec * 32 + lane) * 16 : WS_ML + rec * 16, pack_g(O, tg));
    }
    STAMP(8);
    {
      const int b = lane >> 3, i = lane & 7;
      const u32 oo = WS_O + ((hh * 8 + b) * 4 * 32 + 8 * w + i) * 16;
      const u32 off[5] = {WS_ML + (hh * 32 + b * 4 + (lane & 3)) * 16, oo, oo + 512, oo + 1024, oo + 1536};
      v4u gg[5];
      __builtin_amdgcn_s_sleep(HSLEEP0);
      SPINS(13, (gather<5, HSLEEP>(rs, off, gg, tg)));
      STAMP(9);
      const float ms = u2f(gg[0][0] & 0xfffff000u), ls = u2f(gg[0][1] & 0xfffff000u);
      float M = fmaxf(ms, dppm<0xB1>(ms));
      M = fmaxf(M, dppm<0x4E>(M));
      const float ew = __builtin_amdgcn_exp2f((ms - M) * 1.4426950408889634f);
      float Ls = ew * ls;
      Ls += dppm<0xB1>(Ls);
      Ls += dppm<0x4E>(Ls);
      const float wn = ew * __builtin_amdgcn_rcpf(Ls);
      const f32x4 O = (val_g(gg[1]) * dppm<0x00>(wn) + val_g(gg[2]) * dppm<0x55>(wn)) + (val_g(gg[3]) * dppm<0xAA>(wn) + val_g(gg[4]) * dppm<0xFF>(wn));
      *(LAS v2u*)(L + L_XQ + b * 288 + (32 * w + 4 * i) * 2) = v2u{pk2(O[0], O[1]), pk2(O[2], O[3])};
      wg_barrier();
    }
    if (w == 0) {
      const u32 fo = WS_F + ((((ns * 4 + fnt) * 2 + fG) * 32 + (lane >> 2)) * 16 + 4 * fcq + (lane & 3)) * 16;
      const u32 off[2] = {fo, fo + 16 * 256};
      v4u gg[2];
      __builtin_amdgcn_s_sleep(FSLEEP0);
      SPINS(14, (gather<2, FSLEEP>(rs, off, gg, tg)));
      STAMP(10);
      const f32x4 v = val_g(gg[0]) + val_g(gg[1]);
      auto a02 = __builtin_amdgcn_permlane32_swap(f2u(v[0]), f2u(v[2]), false, false);
      auto a13 = __builtin_amdgcn_permlane32_swap(f2u(v[1]), f2u(v[3]), false, false);
      const u32 p0 = a02[0], p1 = a02[1], p2 = a13[0], p3 = a13[1];
      const float lo = u2f(p0) + u2f(p1), hi = u2f(p2) + u2f(p3);
      auto a = __builtin_amdgcn_permlane16_swap(f2u(lo), f2u(hi), false, false);
      const u32 q0 = a[0], q1 = a[1];
      float t = u2f(q0) + u2f(q1);
      t += dppm<0x124>(t);
      t += dppm<0x128>(t);
      if ((lane & 12) == 0) *(GAS unsigned short*)(p.out + (fb * DM + fcol) * 2) = (unsigned short)pk2(bflo(xres) + t, 0.f);
      if (wg == 0 && lane == 0) __hip_atomic_store((GAS u64*)(p.ws + WS_EPOCH), e, __ATOMIC_RELAXED, __HIP_MEMORY_SCOPE_AGENT);
    }
    STAMPS_OUT();
    __builtin_amdgcn_s_waitcnt(0);
    __builtin_amdgcn_endpgm();
    __builtin_unreachable();
  }
}

}

extern "C" void kernel_launch(void* const* d_in, const int* in_sizes, int n_in, void* d_out, int out_size, void* d_ws,
                              size_t ws_size, hipStream_t stream) {
  (void)in_sizes; (void)n_in; (void)out_size; (void)ws_size;
  Params p;
  p.x = (const char*)d_in[0];
  p.kc = (const char*)d_in[1];
  p.vc = (const char*)d_in[2];
  p.wq = (const char*)d_in[3];
  p.wo = (const char*)d_in[4];
  p.w1 = (const char*)d_in[6];
  p.w2 = (const char*)d_in[7];
  p.out = (char*)d_out;
  p.ws = (char*)d_ws;
  hipLaunchKernelGGL(fused_decode_kernel, dim3(NWG), dim3(768), 0, stream, p);
}
```

```cpp
#define PROBE 11
#define PROBEW 0
#define PROBEINV 0
#include <hip/hip_runtime.h>
#include <stdint.h>

#define GAS __attribute__((address_space(1)))
#define LAS __attribute__((address_space(3)))
typedef unsigned u32;
typedef unsigned long long u64;
typedef u32 v4u __attribute__((ext_vector_type(4)));
typedef u32 v2u __attribute__((ext_vector_type(2)));
typedef float f32x4 __attribute__((ext_vector_type(4)));
typedef float f32x2 __attribute__((ext_vector_type(2)));
typedef short s16x8 __attribute__((ext_vector_type(8)));
typedef short s16x4 __attribute__((ext_vector_type(4)));
typedef __bf16 bf16x2 __attribute__((ext_vector_type(2)));

#define KV_NT 1
#define W_NT 1
#define WIN 2
#define GSLEEP0 16
#define GSLEEP 8
#define ROT 0
#define LPRIO 3
#define FSLEEP0 2
#define FSLEEP 1
#define HSLEEP0 2
#define HSLEEP 2

namespace {

constexpr int DM = 512, HKV = 8, SK = 4096, DH = 128, WINDOW = 1024, FF = 1792;
constexpr int NMAIN = 8, NWAVES = 10, NTHR = NWAVES * 64, NWG = 256;

constexpr u32 WS_EPOCH = 0;
constexpr u32 WS_Q = 4096;
constexpr u32 WS_H = WS_Q + 8 * 8 * 4 * 128 * 16;
constexpr u32 WS_O = WS_H + 28 * 8 * 8 * 16 * 16;
constexpr u32 WS_ML = WS_O + 32 * 8 * 4 * 32 * 16;
constexpr u32 WS_F = WS_ML + 32 * 32 * 16;
constexpr u32 WS_END = WS_F + 8 * 4 * 2 * 32 * 16 * 16;
constexpr u32 WS_ST = WS_END;

constexpr u32 L_K = 0, L_V = 65536, L_W = 131072, L_W1 = L_W + 16384;
constexpr u32 L_XQ = L_W + 24576;
constexpr u32 L_XH = L_XQ + 8 * 288;
constexpr u32 L_QI = L_XH + 8 * 160;
constexpr u32 L_TR = L_QI + 128;
constexpr u32 L_ML = L_TR + 4096;
constexpr u32 L_FLAG = L_ML + 160;
constexpr u32 L_END = L_FLAG + 192;
static_assert(L_END <= 163840, "LDS");
constexpr u32 LDS_BYTES = 163840;

constexpr u32 SPIN_CAP = 1u << 16;

struct Params {
  const char* x; const char* kc; const char* vc; const char* wq; const char* wo; const char* w1; const char* w2;
  char* out; char* ws;
};

__device__ __forceinline__ float bflo(u32 u) { return __builtin_bit_cast(float, u << 16); }
__device__ __forceinline__ float bfhi(u32 u) { return __builtin_bit_cast(float, u & 0xffff0000u); }
__device__ __forceinline__ u32 pk2(float lo, float hi) {
  f32x2 v = {lo, hi};
  return __builtin_bit_cast(u32, __builtin_convertvector(v, bf16x2));
}
__device__ __forceinline__ float rbf(float f) { return bflo(pk2(f, f) & 0xffffu); }

template <int CTRL, int RM> __device__ __forceinline__ float dppz(float v) {
  return __builtin_bit_cast(float, __builtin_amdgcn_update_dpp(0, __builtin_bit_cast(int, v), CTRL, RM, 0xf, true));
}
template <int CTRL> __device__ __forceinline__ float dppm(float v) {
  return __builtin_bit_cast(float, __builtin_amdgcn_mov_dpp(__builtin_bit_cast(int, v), CTRL, 0xf, 0xf, true));
}
__device__ __forceinline__ float wave_sum(float v) {
  v += dppz<0x111, 0xf>(v); v += dppz<0x112, 0xf>(v); v += dppz<0x114, 0xf>(v); v += dppz<0x118, 0xf>(v);
  v += dppz<0x142, 0xa>(v); v += dppz<0x143, 0xc>(v);
  return __builtin_bit_cast(float, __builtin_amdgcn_readlane(__builtin_bit_cast(int, v), 63));
}
__device__ __forceinline__ float u2f(u32 u) { return __builtin_bit_cast(float, u); }
__device__ __forceinline__ u32 f2u(float f) { return __builtin_bit_cast(u32, f); }
__device__ __forceinline__ float x16_32_max(float x) {
  auto s = __builtin_amdgcn_permlane16_swap(f2u(x), f2u(x), false, false);
  const u32 s0 = s[0], s1 = s[1];
  x = fmaxf(u2f(s0), u2f(s1));
  auto t = __builtin_amdgcn_permlane32_swap(f2u(x), f2u(x), false, false);
  const u32 t0 = t[0], t1 = t[1];
  return fmaxf(u2f(t0), u2f(t1));
}
__device__ __forceinline__ float x16_32_sum(float x) {
  auto s = __builtin_amdgcn_permlane16_swap(f2u(x), f2u(x), false, false);
  const u32 s0 = s[0], s1 = s[1];
  x = u2f(s0) + u2f(s1);
  auto t = __builtin_amdgcn_permlane32_swap(f2u(x), f2u(x), false, false);
  const u32 t0 = t[0], t1 = t[1];
  return u2f(t0) + u2f(t1);
}

struct Tag { u32 t0, t1, t2, t3; };
__device__ __forceinline__ u32 pkw(float f, u32 t) { return ((__builtin_bit_cast(u32, f) + 0x800u) & 0xfffff000u) | t; }
__device__ __forceinline__ v4u pack_g(f32x4 v, const Tag& t) { return v4u{pkw(v[0], t.t0), pkw(v[1], t.t1), pkw(v[2], t.t2), pkw(v[3], t.t3)}; }
__device__ __forceinline__ u32 bad_g(v4u g, const Tag& t) { return ((g[0] ^ t.t0) | (g[1] ^ t.t1) | (g[2] ^ t.t2) | (g[3] ^ t.t3)) & 0xfffu; }
__device__ __forceinline__ f32x4 val_g(v4u g) {
  return f32x4{__builtin_bit_cast(float, g[0] & 0xfffff000u), __builtin_bit_cast(float, g[1] & 0xfffff000u),
               __builtin_bit_cast(float, g[2] & 0xfffff000u), __builtin_bit_cast(float, g[3] & 0xfffff000u)};
}
constexpr int AUX_SC1 = 16;
__device__ __forceinline__ v4u ld_g(__amdgpu_buffer_rsrc_t rs, u32 off) { return __builtin_amdgcn_raw_buffer_load_b128(rs, off, 0, AUX_SC1); }
__device__ __forceinline__ void st_g(__amdgpu_buffer_rsrc_t rs, u32 off, v4u v) { __builtin_amdgcn_raw_buffer_store_b128(v, rs, off, 0, AUX_SC1); }

template <int NT> __device__ __forceinline__ void glds(const char* sbase, u32 voff, u32 dst) {
  if constexpr (NT)
    asm volatile("s_mov_b32 m0, %2\n\ts_nop 0\n\tglobal_load_lds_dwordx4 %0, %1 nt" ::"v"(voff), "s"(sbase), "s"(dst) : "memory");
  else
    asm volatile("s_mov_b32 m0, %2\n\ts_nop 0\n\tglobal_load_lds_dwordx4 %0, %1" ::"v"(voff), "s"(sbase), "s"(dst) : "memory");
}
template <int N> __device__ __forceinline__ void waitvm() { asm volatile("s_waitcnt vmcnt(%0)" ::"i"(N) : "memory"); }
__device__ __forceinline__ void wg_barrier() { asm volatile("s_waitcnt lgkmcnt(0)\n\ts_barrier" ::: "memory"); }

__device__ __forceinline__ f32x4 mfma(v4u a, v4u b, f32x4 c) {
  return __builtin_amdgcn_mfma_f32_16x16x32_bf16(__builtin_bit_cast(s16x8, a), __builtin_bit_cast(s16x8, b), c, 0, 0, 0);
}
__device__ __forceinline__ v2u tr_rd(LAS char* L, u32 a) {
  const s16x4 v = __builtin_amdgcn_ds_read_tr16_b64_v4i16((LAS s16x4*)(L + a));
  return __builtin_bit_cast(v2u, v);
}
__device__ __forceinline__ v4u tr_frag(LAS char* L, u32 a0, u32 a1) {
  const v2u x = tr_rd(L, a0), y = tr_rd(L, a1);
  return v4u{x[0], x[1], y[0], y[1]};
}
__device__ __forceinline__ v4u lds128(LAS char* L, u32 a) { return *(LAS v4u*)(L + a); }

#define NSTAMP 16
#define STAMP(k) do { if ((k) == PROBE || (k) == 0) asm volatile("s_memrealtime %0\n\ts_waitcnt lgkmcnt(0)" : "=s"(stamps[k])::"memory"); } while (0)
#define STAMPS_OUT() do { if (wave == PROBEW && stamps[PROBE] != 0) { u64 now_; asm volatile("s_memrealtime %0\n\ts_waitcnt lgkmcnt(0)" : "=s"(now_)::"memory"); \
    const long long dt_ = (long long)(stamps[PROBE] - stamps[0]); const u64 until_ = now_ + (u64)(PROBEINV ? 10 * (4000 - dt_) : 10 * dt_); \
    do { __builtin_amdgcn_s_sleep(4); asm volatile("s_memrealtime %0\n\ts_waitcnt lgkmcnt(0)" : "=s"(now_)::"memory"); } while (now_ < until_); } } while (0)
#define SPINS(k, e) do { (void)(e); } while (0)
#define STAMPS_DECL 1

constexpr u32 C_A = L_FLAG, C_P = L_FLAG + 4, C_Q = L_FLAG + 8, C_M = L_FLAG + 12, C_W2 = L_FLAG + 16;
constexpr u32 F_K = L_FLAG + 32, F_P = L_FLAG + 64;
constexpr u32 L_TAG = L_FLAG + 96;
constexpr u32 L_MX = L_FLAG + 128;
static_assert(L_MX + 64 <= 163840 && L_FLAG % 16 == 0 && L_ML % 16 == 0, "LDS");

__device__ __forceinline__ u32 lds_ld(LAS char* L, u32 a) { return *(volatile LAS u32*)(L + a); }
__device__ __forceinline__ void lds_st(LAS char* L, u32 a, u32 v) { *(volatile LAS u32*)(L + a) = v; }
__device__ __forceinline__ void lds_arrive(LAS char* L, u32 a, int lane) {
  asm volatile("s_waitcnt lgkmcnt(0)" ::: "memory");
  if (lane == 0) __hip_atomic_fetch_add((LAS u32*)(L + a), 1u, __ATOMIC_RELAXED, __HIP_MEMORY_SCOPE_WORKGROUP);
}
__device__ __forceinline__ void lds_flag(LAS char* L, u32 a, int lane) {
  asm volatile("s_waitcnt lgkmcnt(0)" ::: "memory");
  if (lane == 0) lds_st(L, a, 1u);
}
template <int SLEEP> __device__ __forceinline__ void lds_wait(LAS char* L, u32 a, u32 n) {
  while (lds_ld(L, a) < n) { if (SLEEP) __builtin_amdgcn_s_sleep(SLEEP); }
  asm volatile("" ::: "memory");
}
__device__ __forceinline__ Tag make_tag(u64 e) {
  Tag t; t.t0 = (u32)e & 0xfffu; t.t1 = (u32)(e >> 12) & 0xfffu; t.t2 = t.t0; t.t3 = (u32)(e >> 24) & 0xfffu; return t;
}
__device__ __forceinline__ u32 chk(u32 acc, v4u g, const Tag& t) {
  acc = __builtin_amdgcn_bitop3_b32(acc, g[0], t.t0, 0xF6);
  acc = __builtin_amdgcn_bitop3_b32(acc, g[1], t.t1, 0xF6);
  acc = __builtin_amdgcn_bitop3_b32(acc, g[2], t.t2, 0xF6);
  acc = __builtin_amdgcn_bitop3_b32(acc, g[3], t.t3, 0xF6);
  return acc;
}
template <int N, int SLEEP> __device__ __forceinline__ u32 gather(__amdgpu_buffer_rsrc_t rs, const u32 (&off)[N], v4u (&g)[N], const Tag& tg) {
  u32 spin = 0;
  while (true) {
#pragma unroll
    for (int i = 0; i < N; ++i) g[i] = ld_g(rs, off[i]);
    u32 acc0 = 0, acc1 = 0;
#pragma unroll
    for (int i = 0; i < N; ++i) { if (i & 1) acc1 = chk(acc1, g[i], tg); else acc0 = chk(acc0, g[i], tg); }
    if (__all(((acc0 | acc1) & 0xfffu) == 0u) || ++spin > SPIN_CAP) break;
    if (SLEEP) __builtin_amdgcn_s_sleep(SLEEP);
    asm volatile("" ::: "memory");
  }
  return spin;
}

__global__ void __launch_bounds__(768, 1) fused_decode_kernel(Params p) {
  __shared__ __attribute__((aligned(16))) char lds_raw[LDS_BYTES];
  LAS char* L = (LAS char*)lds_raw;
  const u32 lbase = (u32)(uintptr_t)L;
  const int tid = threadIdx.x, lane = tid & 63;
  const int wave = __builtin_amdgcn_readfirstlane(tid >> 6);
  const int wg = blockIdx.x, xx = (wg + ROT) & 7, j = wg >> 3;
  const int g = lane >> 4;
  u64 stamps[NSTAMP];
#pragma unroll
  for (int k = 0; k < NSTAMP; ++k) stamps[k] = 0;
  asm volatile("s_memrealtime %0" : "=s"(stamps[0]));
  asm volatile("s_waitcnt lgkmcnt(0)" : "+s"(stamps[0]) : "s"(p.x), "s"(p.kc), "s"(p.vc), "s"(p.wq), "s"(p.wo), "s"(p.w1), "s"(p.w2), "s"(p.out), "s"(p.ws));
#define STAMP0_DONE
  const int cs = j >> 2, ks = j & 3;
  const int hh = 4 * xx + (j >> 3), ns = j & 7;
  const int bb = j >> 2, sp = j & 3;
  const bool ffn = xx < 7;
  const u32 trq = (u32)((lane >> 2) & 3), trp = (u32)(lane & 3);
  const u32 trf = ((trq >> 1) & 1) | (((u32)g & 1) << 1);
  const u32 tr_row = ((u32)g * 8 + trq) * 128 + trp * 8;

  if (wave >= 4) {
    __builtin_amdgcn_s_setprio(LPRIO);
    const int w = wave - 4;
    const u32 wchunk = (u32)((lane & 7) ^ (((lane >> 4) & 1) << 1));
    const u32 wrow = (u32)(lane >> 3);
    {
      const char* b0 = p.wq + ((size_t)(ks * 128 + 16 * w) * 4096 + xx * 512 + cs * 64) * 2;
      const u32 vo = wrow * 8192 + wchunk * 16;
      glds<W_NT>(b0, vo, lbase + L_W + (2 * w) * 1024);
      glds<W_NT>(b0 + 8 * 8192, vo ^ 64u, lbase + L_W + (2 * w + 1) * 1024);
      if (ffn) {
        const char* b1 = p.w1 + ((size_t)(ns * 64 + 8 * w) * FF + hh * 64) * 2;
        const u32 vo1 = wrow * (FF * 2) + ((wchunk ^ ((u32)(w & 1) << 2)) * 16);
        glds<W_NT>(b1, vo1, lbase + L_W1 + w * 1024);
      }
    }
    wg_barrier();
    const size_t kvoff = ((((size_t)bb * HKV + xx) * SK + (SK - WINDOW) + sp * 256 + w * 32) * DH) * 2;
    const char* kb = p.kc + kvoff;
    const char* vb = p.vc + kvoff;
    const u32 r4 = (u32)(lane >> 4), c16 = (u32)(lane & 15);
    const u32 kvo = r4 * 256 + ((c16 ^ r4) * 16);
    const u32 vvo = r4 * 256 + ((c16 ^ (r4 << 1)) * 16);
    auto issueK = [&](int u) { const int pu = (u + j) & 7; glds<KV_NT>(kb + pu * 1024, kvo ^ (u32)((pu & 3) << 6), lbase + L_K + w * 8192 + pu * 1024); };
    auto issueV = [&](int u) { const int pu = (u + j) & 7; glds<KV_NT>(vb + pu * 1024, vvo ^ (u32)((pu & 1) << 7), lbase + L_V + w * 8192 + pu * 1024); };
    issueK(0); issueK(1); issueK(2);
    waitvm<3>();
    lds_arrive(L, C_A, lane);
    STAMP(1);
    issueK(3); waitvm<WIN>();
    issueK(4); waitvm<WIN>();
    issueK(5); waitvm<WIN>();
    issueK(6); waitvm<WIN>();
    issueK(7); waitvm<WIN>();
    issueV(0); waitvm<WIN>();
    issueV(1); waitvm<2>();
    lds_flag(L, F_K + w * 4, lane);
    STAMP(2);
    issueV(2); waitvm<WIN>();
    issueV(3); waitvm<WIN>();
    issueV(4); waitvm<WIN>();
    issueV(5); waitvm<WIN>();
    issueV(6); waitvm<WIN>();
    issueV(7); waitvm<WIN>();
    lds_wait<1>(L, C_P, 4);
    {
      const char* b0 = p.wo + ((size_t)(hh * 128 + 16 * w) * DM + ns * 64) * 2;
      const u32 vo = wrow * 1024 + wchunk * 16;
      glds<W_NT>(b0, vo, lbase + L_W + (2 * w) * 1024); waitvm<WIN>();
      glds<W_NT>(b0 + 8 * 1024, vo ^ 64u, lbase + L_W + (2 * w + 1) * 1024);
      if (ffn) {
        const char* b1 = p.w2 + ((size_t)(hh * 64 + 8 * w) * DM + ns * 64) * 2;
        glds<W_NT>(b1, wrow * 1024 + ((wchunk ^ ((u32)(w & 1) << 2)) * 16), lbase + L_W1 + w * 1024);
      }
    }
    STAMP(3);
    lds_wait<1>(L, F_P + w * 4, 1);
    const v4u P = lds128(L, L_K + w * 8192 + 4096 + g * 64 + (lane & 3) * 16);
    const u32 vrow = (u32)g * 4 + trq;
    const u32 va = L_V + w * 8192 + vrow * 256 + ((vrow & 7) * 32) + trp * 8;
    STAMP(4);
    if (ffn) waitvm<3>(); else waitvm<2>();
    STAMP(5);
    {
      v4u A[8];
#pragma unroll
      for (int dt = 0; dt < 8; ++dt) { const u32 a = va ^ (u32)(dt << 5); A[dt] = tr_frag(L, a, a + 4096); }
      f32x4 o[8];
#pragma unroll
      for (int dt = 0; dt < 8; ++dt) o[dt] = mfma(A[dt], P, f32x4{0.f, 0.f, 0.f, 0.f});
      if ((lane & 15) < 4) {
#pragma unroll
        for (int dt = 0; dt < 8; ++dt) *(LAS f32x4*)(L + L_K + w * 8192 + (lane & 3) * 512 + dt * 64 + g * 16) = o[dt];
      }
    }
    wg_barrier();
    STAMP(6);
    waitvm<0>();
    lds_arrive(L, C_W2, lane);
    if (w < 4) {
      const __amdgpu_buffer_rsrc_t rs = __builtin_amdgcn_make_buffer_rsrc((void*)p.ws, 0, (int)WS_END, 0x00020000);
      const v4u tv = lds128(L, L_TAG);
      const Tag tg{tv[0], tv[1], tv[2], tv[3]};
      lds_wait<1>(L, C_W2, 8);
      STAMP(7);
      const int nt = w;
      const u32 a0 = tr_row + (((u32)nt ^ trf) * 32);
      v4u A[4];
#pragma unroll
      for (int t = 0; t < 4; ++t) A[t] = tr_frag(L, L_W + a0 + t * 4096, L_W + a0 + t * 4096 + 512);
      f32x4 acc = {0.f, 0.f, 0.f, 0.f};
      if (ffn) {
#pragma unroll
        for (int t = 0; t < 2; ++t) {
          const v4u A2 = tr_frag(L, L_W1 + a0 + t * 4096, L_W1 + a0 + t * 4096 + 512);
          const v4u B = lds128(L, L_XH + (lane & 7) * 160 + (4 * t + g) * 16);
          acc = mfma(B, A2, acc);
        }
      }
      wg_barrier();
      STAMP(8);
#pragma unroll
      for (int t = 0; t < 4; ++t) {
        const v4u B = lds128(L, L_XQ + (lane & 7) * 288 + (4 * t + g) * 16);
        acc = mfma(B, A[t], acc);
      }
      if (lane < 32) st_g(rs, WS_F + ((((ns * 4 + nt) * 2 + g) * 32 + hh) * 16 + (lane & 15)) * 16, pack_g(acc, tg));
      STAMP(9);
    }
    STAMPS_OUT();
    __builtin_amdgcn_s_waitcnt(0);
    __builtin_amdgcn_endpgm();
    __builtin_unreachable();
  } else {
    const int w = wave;
    const __amdgpu_buffer_rsrc_t rs = __builtin_amdgcn_make_buffer_rsrc((void*)p.ws, 0, (int)WS_END, 0x00020000);
    const int fnt = hh & 3, fG = (hh >> 2) & 1, fcq = hh >> 3;
    const int fb = 4 * fG + 2 * (lane >> 5) + ((lane >> 4) & 1);
    const int fcol = ns * 64 + fnt * 16 + 4 * fcq + (lane & 3);
    v4u xv[2];
    v2u ev;
    u32 xres;
    {
      const u32 xo = (u32)(2 * w * 1024 + lane * 16), zero = 0, ro = (u32)(fb * DM + fcol) * 2;
      asm volatile("global_load_dwordx4 %0, %1, %2" : "=v"(xv[0]) : "v"(xo), "s"(p.x) : "memory");
      asm volatile("global_load_dwordx4 %0, %1, %2 offset:1024" : "=v"(xv[1]) : "v"(xo), "s"(p.x) : "memory");
      asm volatile("global_load_dwordx2 %0, %1, %2 sc1" : "=v"(ev) : "v"(zero), "s"(p.ws) : "memory");
      asm volatile("global_load_ushort %0, %1, %2" : "=v"(xres) : "v"(ro), "s"(p.x) : "memory");
    }
    if (w == 3 && lane < 32) lds_st(L, L_FLAG + lane * 4, 0u);
    wg_barrier();
    asm volatile("s_waitcnt vmcnt(2)" : "+v"(xv[0]), "+v"(xv[1])::"memory");
    STAMP(11);
    {
      float v[2][8], s1[2], s2[2], piv[2];
#pragma unroll
      for (int r = 0; r < 2; ++r) {
#pragma unroll
        for (int q = 0; q < 4; ++q) { v[r][2 * q] = bflo(xv[r][q]); v[r][2 * q + 1] = bfhi(xv[r][q]); }
        piv[r] = u2f(__builtin_amdgcn_readfirstlane(f2u(v[r][0])));
        float d[8];
#pragma unroll
        for (int q = 0; q < 8; ++q) d[q] = v[r][q] - piv[r];
        s1[r] = ((d[0] + d[1]) + (d[2] + d[3])) + ((d[4] + d[5]) + (d[6] + d[7]));
        s2[r] = fmaf(d[1], d[1], d[0] * d[0]) + fmaf(d[3], d[3], d[2] * d[2]) + (fmaf(d[5], d[5], d[4] * d[4]) + fmaf(d[7], d[7], d[6] * d[6]));
      }
#define RED4(C, RM) { const float a = dppz<C, RM>(s1[0]), b = dppz<C, RM>(s2[0]), c = dppz<C, RM>(s1[1]), d = dppz<C, RM>(s2[1]); s1[0] += a; s2[0] += b; s1[1] += c; s2[1] += d; }
      RED4(0x111, 0xf) RED4(0x112, 0xf) RED4(0x114, 0xf) RED4(0x118, 0xf) RED4(0x142, 0xa) RED4(0x143, 0xc)
#undef RED4
#pragma unroll
      for (int r = 0; r < 2; ++r) {
        const float t1 = u2f(__builtin_amdgcn_readlane(f2u(s1[r]), 63)), t2 = u2f(__builtin_amdgcn_readlane(f2u(s2[r]), 63));
        const float md = t1 * (1.f / DM);
        const float var = rbf(fmaxf(fmaf(-md, md, t2 * (1.f / DM)), 0.f));
        const float mean = rbf(piv[r] + md);
        const float den = rbf(__builtin_amdgcn_sqrtf(rbf(var + 1e-5f)));
        const float rd = __builtin_amdgcn_rcpf(den);
        v4u xn;
#pragma unroll
        for (int q = 0; q < 4; ++q) {
          const u32 d2 = pk2(v[r][2 * q] - mean, v[r][2 * q + 1] - mean);
          xn[q] = pk2(bflo(d2) * rd, bfhi(d2) * rd);
        }
        if ((lane >> 4) == ks) *(LAS v4u*)(L + L_XQ + (2 * w + r) * 288 + (lane & 15) * 16) = xn;
        if ((lane >> 3) == ns) *(LAS v4u*)(L + L_XH + (2 * w + r) * 160 + (lane & 7) * 16) = xn;
      }
    }
    asm volatile("s_waitcnt vmcnt(0)" : "+v"(ev), "+v"(xres)::"memory");
    const u64 e = (((u64)(u32)__builtin_amdgcn_readfirstlane(ev[1]) << 32) | (u32)__builtin_amdgcn_readfirstlane(ev[0])) + 1;
    const Tag tg = make_tag(e);
    if (w == 0 && lane == 0) *(LAS v4u*)(L + L_TAG) = v4u{tg.t0, tg.t1, tg.t2, tg.t3};
    lds_arrive(L, C_A, lane);
    STAMP(1);
    lds_wait<1>(L, C_A, 12);
    STAMP(2);
    {
      const u32 a0 = tr_row + (((u32)w ^ trf) * 32);
      v4u A[4], B[4], A2[2], B2[2];
#pragma unroll
      for (int t = 0; t < 4; ++t) { A[t] = tr_frag(L, L_W + a0 + t * 4096, L_W + a0 + t * 4096 + 512); B[t] = lds128(L, L_XQ + (lane & 7) * 288 + (4 * t + g) * 16); }
      if (ffn) {
#pragma unroll
        for (int t = 0; t < 2; ++t) { A2[t] = tr_frag(L, L_W1 + a0 + t * 4096, L_W1 + a0 + t * 4096 + 512); B2[t] = lds128(L, L_XH + (lane & 7) * 160 + (4 * t + g) * 16); }
      }
      f32x4 acc = {0.f, 0.f, 0.f, 0.f}, acc2 = {0.f, 0.f, 0.f, 0.f};
#pragma unroll
      for (int t = 0; t < 4; ++t) acc = mfma(A[t], B[t], acc);
      if (ffn) {
#pragma unroll
        for (int t = 0; t < 2; ++t) acc2 = mfma(A2[t], B2[t], acc2);
      }
      LAS char* T = L + L_TR + w * 1024;
      if ((lane & 15) < 8) { *(LAS f32x4*)(T + (lane & 7) * 64 + g * 16) = acc; *(LAS f32x4*)(T + 512 + (lane & 7) * 64 + g * 16) = acc2; }
      const f32x4 tq = *(LAS f32x4*)(T + lane * 16);
      const int tb = (lane >> 2) & 7, tG = lane & 3;
      const u32 oq = WS_Q + (((xx * 8 + tb) * 4 + ks) * 128 + cs * 16 + w * 4 + tG) * 16;
      const u32 oh = WS_H + (((hh * 8 + ns) * 8 + tb) * 16 + w * 4 + tG) * 16;
      if (lane < 32 || ffn) st_g(rs, lane < 32 ? oq : oh, pack_g(tq, tg));
    }
    lds_arrive(L, C_P, lane);
    STAMP(3);
    if (w < 2) {
      __builtin_amdgcn_s_sleep(GSLEEP0);
      const u32 qo = WS_Q + ((xx * 8 + bb) * 4 * 128 + w * 64 + lane) * 16;
      const u32 off[4] = {qo, qo + 2048, qo + 4096, qo + 6144};
      v4u ga[4];
      SPINS(12, (gather<4, GSLEEP>(rs, off, ga, tg)));
      const f32x4 a = (val_g(ga[0]) + val_g(ga[1])) + (val_g(ga[2]) + val_g(ga[3]));
      *(LAS v2u*)(L + L_QI + (2 * w + (lane >> 5)) * 288 + (lane & 31) * 8) = v2u{pk2(a[0], a[1]), pk2(a[2], a[3])};
      lds_arrive(L, C_Q, lane);
    } else if (ffn) {
      lds_wait<1>(L, C_P, 4);
      __builtin_amdgcn_s_sleep(GSLEEP0);
      const u32 ho = WS_H + (hh * 8 * 128 + (w - 2) * 64 + lane) * 16;
      u32 off[8];
#pragma unroll
      for (int s = 0; s < 8; ++s) off[s] = ho + s * 2048;
      v4u ga[8];
      SPINS(12, (gather<8, GSLEEP>(rs, off, ga, tg)));
      f32x4 a = ((val_g(ga[0]) + val_g(ga[1])) + (val_g(ga[2]) + val_g(ga[3]))) + ((val_g(ga[4]) + val_g(ga[5])) + (val_g(ga[6]) + val_g(ga[7])));
      float r[4];
#pragma unroll
      for (int q = 0; q < 4; ++q) { const float hv = rbf(a[q]); r[q] = hv * __builtin_amdgcn_rcpf(1.f + __builtin_amdgcn_exp2f(-hv * 1.4426950408889634f)); }
      *(LAS v2u*)(L + L_XH + (4 * (w - 2) + (lane >> 4)) * 160 + (lane & 15) * 8) = v2u{pk2(r[0], r[1]), pk2(r[2], r[3])};
    }
    STAMP(4);
    lds_wait<1>(L, C_Q, 2);
    lds_wait<1>(L, F_K + (2 * w) * 4, 1);
    lds_wait<1>(L, F_K + (2 * w + 1) * 4, 1);
    STAMP(5);
    {
      const u32 c16 = (u32)(lane & 15);
      float s[2][8], mw[2];
      v4u B[4];
#pragma unroll
      for (int t = 0; t < 4; ++t) B[t] = lds128(L, L_QI + (lane & 3) * 288 + (4 * t + g) * 16);
#pragma unroll
      for (int u = 0; u < 2; ++u) {
        f32x4 s0 = {0.f, 0.f, 0.f, 0.f}, s1 = {0.f, 0.f, 0.f, 0.f};
        const u32 ka = L_K + (2 * w + u) * 8192 + c16 * 256 + (((u32)g ^ c16) * 16);
        v4u A0[4], A1[4];
#pragma unroll
        for (int t = 0; t < 4; ++t) { A0[t] = lds128(L, ka ^ (u32)(t << 6)); A1[t] = lds128(L, (ka ^ (u32)(t << 6)) + 4096); }
#pragma unroll
        for (int t = 0; t < 4; ++t) { s0 = mfma(A0[t], B[t], s0); s1 = mfma(A1[t], B[t], s1); }
#pragma unroll
        for (int r = 0; r < 4; ++r) { s[u][r] = rbf(rbf(s0[r]) * 0.08838834764831845f); s[u][4 + r] = rbf(rbf(s1[r]) * 0.08838834764831845f); }
        mw[u] = fmaxf(fmaxf(fmaxf(s[u][0], s[u][1]), fmaxf(s[u][2], s[u][3])), fmaxf(fmaxf(s[u][4], s[u][5]), fmaxf(s[u][6], s[u][7])));
      }
      const float m = x16_32_max(fmaxf(mw[0], mw[1]));
      if (lane < 4) *(LAS float*)(L + L_MX + (lane * 4 + w) * 4) = m;
      lds_arrive(L, C_M, lane);
      lds_wait<1>(L, C_M, 4);
      const f32x4 m0 = *(LAS f32x4*)(L + L_MX + (lane & 3) * 16);
      const float M = fmaxf(fmaxf(m0[0], m0[1]), fmaxf(m0[2], m0[3]));
      float l = 0.f;
#pragma unroll
      for (int u = 0; u < 2; ++u) {
        float ex[8];
#pragma unroll
        for (int r = 0; r < 8; ++r) { ex[r] = __builtin_amdgcn_exp2f((s[u][r] - M) * 1.4426950408889634f); l += ex[r]; }
        if ((lane & 15) < 4) *(LAS v4u*)(L + L_K + (2 * w + u) * 8192 + 4096 + g * 64 + (lane & 3) * 16) = v4u{pk2(ex[0], ex[1]), pk2(ex[2], ex[3]), pk2(ex[4], ex[5]), pk2(ex[6], ex[7])};
      }
      l = x16_32_sum(l);
      if (lane < 4) { *(LAS float*)(L + L_ML + (lane * 4 + w) * 4) = l; if (w == 0) *(LAS float*)(L + L_ML + 128 + lane * 4) = M; }
      asm volatile("s_waitcnt lgkmcnt(0)" ::: "memory");
      if (lane < 2) lds_st(L, F_P + (2 * w + lane) * 4, 1u);
    }
    STAMP(6);
    wg_barrier();
    STAMP(7);
    {
      const int dq = lane & 31;
      f32x4 o[8];
#pragma unroll
      for (int u = 0; u < 8; ++u) o[u] = *(LAS f32x4*)(L + L_K + u * 8192 + w * 512 + dq * 16);
      const f32x4 l0 = *(LAS f32x4*)(L + L_ML + w * 16);
      const float M = *(LAS float*)(L + L_ML + 128 + w * 4);
      f32x4 O = ((o[0] + o[1]) + (o[2] + o[3])) + ((o[4] + o[5]) + (o[6] + o[7]));
      if (lane >= 32) O = f32x4{M, (l0[0] + l0[1]) + (l0[2] + l0[3]), 0.f, 0.f};
      const int rec = ((4 * xx + w) * 8 + bb) * 4 + sp;
      if (lane < 33) st_g(rs, lane < 32 ? WS_O + (rec * 32 + lane) * 16 : WS_ML + rec * 16, pack_g(O, tg));
    }
    STAMP(8);
    {
      const int b = lane >> 3, i = lane & 7;
      const u32 oo = WS_O + ((hh * 8 + b) * 4 * 32 + 8 * w + i) * 16;
      const u32 off[5] = {WS_ML + (hh * 32 + b * 4 + (lane & 3)) * 16, oo, oo + 512, oo + 1024, oo + 1536};
      v4u gg[5];
      __builtin_amdgcn_s_sleep(HSLEEP0);
      SPINS(13, (gather<5, HSLEEP>(rs, off, gg, tg)));
      STAMP(9);
      const float ms = u2f(gg[0][0] & 0xfffff000u), ls = u2f(gg[0][1] & 0xfffff000u);
      float M = fmaxf(ms, dppm<0xB1>(ms));
      M = fmaxf(M, dppm<0x4E>(M));
      const float ew = __builtin_amdgcn_exp2f((ms - M) * 1.4426950408889634f);
      float Ls = ew * ls;
      Ls += dppm<0xB1>(Ls);
      Ls += dppm<0x4E>(Ls);
      const float wn = ew * __builtin_amdgcn_rcpf(Ls);
      const f32x4 O = (val_g(gg[1]) * dppm<0x00>(wn) + val_g(gg[2]) * dppm<0x55>(wn)) + (val_g(gg[3]) * dppm<0xAA>(wn) + val_g(gg[4]) * dppm<0xFF>(wn));
      *(LAS v2u*)(L + L_XQ + b * 288 + (32 * w + 4 * i) * 2) = v2u{pk2(O[0], O[1]), pk2(O[2], O[3])};
      wg_barrier();
    }
    if (w == 0) {
      const u32 fo = WS_F + ((((ns * 4 + fnt) * 2 + fG) * 32 + (lane >> 2)) * 16 + 4 * fcq + (lane & 3)) * 16;
      const u32 off[2] = {fo, fo + 16 * 256};
      v4u gg[2];
      __builtin_amdgcn_s_sleep(FSLEEP0);
      SPINS(14, (gather<2, FSLEEP>(rs, off, gg, tg)));
      STAMP(10);
      const f32x4 v = val_g(gg[0]) + val_g(gg[1]);
      auto a02 = __builtin_amdgcn_permlane32_swap(f2u(v[0]), f2u(v[2]), false, false);
      auto a13 = __builtin_amdgcn_permlane32_swap(f2u(v[1]), f2u(v[3]), false, false);
      const u32 p0 = a02[0], p1 = a02[1], p2 = a13[0], p3 = a13[1];
      const float lo = u2f(p0) + u2f(p1), hi = u2f(p2) + u2f(p3);
      auto a = __builtin_amdgcn_permlane16_swap(f2u(lo), f2u(hi), false, false);
      const u32 q0 = a[0], q1 = a[1];
      float t = u2f(q0) + u2f(q1);
      t += dppm<0x124>(t);
      t += dppm<0x128>(t);
      if ((lane & 12) == 0) *(GAS unsigned short*)(p.out + (fb * DM + fcol) * 2) = (unsigned short)pk2(bflo(xres) + t, 0.f);
      if (wg == 0 && lane == 0) __hip_atomic_store((GAS u64*)(p.ws + WS_EPOCH), e, __ATOMIC_RELAXED, __HIP_MEMORY_SCOPE_AGENT);
    }
    STAMP(15);
    STAMPS_OUT();
    __builtin_amdgcn_s_waitcnt(0);
    __builtin_amdgcn_endpgm();
    __builtin_unreachable();
  }
}

}

extern "C" void kernel_launch(void* const* d_in, const int* in_sizes, int n_in, void* d_out, int out_size, void* d_ws,
                              size_t ws_size, hipStream_t stream) {
  (void)in_sizes; (void)n_in; (void)out_size; (void)ws_size;
  Params p;
  p.x = (const char*)d_in[0];
  p.kc = (const char*)d_in[1];
  p.vc = (const char*)d_in[2];
  p.wq = (const char*)d_in[3];
  p.wo = (const char*)d_in[4];
  p.w1 = (const char*)d_in[6];
  p.w2 = (const char*)d_in[7];
  p.out = (char*)d_out;
  p.ws = (char*)d_ws;
  hipLaunchKernelGGL(fused_decode_kernel, dim3(NWG), dim3(768), 0, stream, p);
}
```

```cpp
#include <hip/hip_runtime.h>
#include <stdint.h>

#define GAS __attribute__((address_space(1)))
#define LAS __attribute__((address_space(3)))
typedef unsigned u32;
typedef unsigned long long u64;
typedef u32 v4u __attribute__((ext_vector_type(4)));
typedef u32 v2u __attribute__((ext_vector_type(2)));
typedef float f32x4 __attribute__((ext_vector_type(4)));
typedef float f32x2 __attribute__((ext_vector_type(2)));
typedef short s16x8 __attribute__((ext_vector_type(8)));
typedef short s16x4 __attribute__((ext_vector_type(4)));
typedef __bf16 bf16x2 __attribute__((ext_vector_type(2)));

#define KV_NT 1
#define W_NT 1
#define WIN 2
#define GSLEEP0 16
#define GSLEEP 8
#define ROT 0
#define LPRIO 3
#define FSLEEP0 2
#define FSLEEP 1
#define HSLEEP0 2
#define HSLEEP 2

namespace {

constexpr int DM = 512, HKV = 8, SK = 4096, DH = 128, WINDOW = 1024, FF = 1792;
constexpr int NMAIN = 8, NWAVES = 10, NTHR = NWAVES * 64, NWG = 256;

constexpr u32 WS_EPOCH = 0;
constexpr u32 WS_Q = 4096;
constexpr u32 WS_H = WS_Q + 8 * 8 * 4 * 128 * 16;
constexpr u32 WS_O = WS_H + 28 * 8 * 8 * 16 * 16;
constexpr u32 WS_ML = WS_O + 32 * 8 * 4 * 32 * 16;
constexpr u32 WS_F = WS_ML + 32 * 32 * 16;
constexpr u32 WS_END = WS_F + 8 * 4 * 2 * 32 * 16 * 16;
constexpr u32 WS_ST = WS_END;

constexpr u32 L_K = 0, L_V = 65536, L_W = 131072, L_W1 = L_W + 16384;
constexpr u32 L_XQ = L_W + 24576;
constexpr u32 L_XH = L_XQ + 8 * 288;
constexpr u32 L_QI = L_XH + 8 * 160;
constexpr u32 L_TR = L_QI + 128;
constexpr u32 L_ML = L_TR + 4096;
constexpr u32 L_FLAG = L_ML + 160;
constexpr u32 L_END = L_FLAG + 192;
static_assert(L_END <= 163840, "LDS");
constexpr u32 LDS_BYTES = 163840;

constexpr u32 SPIN_CAP = 1u << 16;

struct Params {
  const char* x; const char* kc; const char* vc; const char* wq; const char* wo; const char* w1; const char* w2;
  char* out; char* ws;
};

__device__ __forceinline__ float bflo(u32 u) { return __builtin_bit_cast(float, u << 16); }
__device__ __forceinline__ float bfhi(u32 u) { return __builtin_bit_cast(float, u & 0xffff0000u); }
__device__ __forceinline__ u32 pk2(float lo, float hi) {
  f32x2 v = {lo, hi};
  return __builtin_bit_cast(u32, __builtin_convertvector(v, bf16x2));
}
__device__ __forceinline__ float rbf(float f) { return bflo(pk2(f, f) & 0xffffu); }

template <int CTRL, int RM> __device__ __forceinline__ float dppz(float v) {
  return __builtin_bit_cast(float, __builtin_amdgcn_update_dpp(0, __builtin_bit_cast(int, v), CTRL, RM, 0xf, true));
}
template <int CTRL> __device__ __forceinline__ float dppm(float v) {
  return __builtin_bit_cast(float, __builtin_amdgcn_mov_dpp(__builtin_bit_cast(int, v), CTRL, 0xf, 0xf, true));
}
__device__ __forceinline__ float wave_sum(float v) {
  v += dppz<0x111, 0xf>(v); v += dppz<0x112, 0xf>(v); v += dppz<0x114, 0xf>(v); v += dppz<0x118, 0xf>(v);
  v += dppz<0x142, 0xa>(v); v += dppz<0x143, 0xc>(v);
  return __builtin_bit_cast(float, __builtin_amdgcn_readlane(__builtin_bit_cast(int, v), 63));
}
__device__ __forceinline__ float u2f(u32 u) { return __builtin_bit_cast(float, u); }
__device__ __forceinline__ u32 f2u(float f) { return __builtin_bit_cast(u32, f); }
__device__ __forceinline__ float x16_32_max(float x) {
  auto s = __builtin_amdgcn_permlane16_swap(f2u(x), f2u(x), false, false);
  const u32 s0 = s[0], s1 = s[1];
  x = fmaxf(u2f(s0), u2f(s1));
  auto t = __builtin_amdgcn_permlane32_swap(f2u(x), f2u(x), false, false);
  const u32 t0 = t[0], t1 = t[1];
  return fmaxf(u2f(t0), u2f(t1));
}
__device__ __forceinline__ float x16_32_sum(float x) {
  auto s = __builtin_amdgcn_permlane16_swap(f2u(x), f2u(x), false, false);
  const u32 s0 = s[0], s1 = s[1];
  x = u2f(s0) + u2f(s1);
  auto t = __builtin_amdgcn_permlane32_swap(f2u(x), f2u(x), false, false);
  const u32 t0 = t[0], t1 = t[1];
  return u2f(t0) + u2f(t1);
}

struct Tag { u32 t0, t1, t2, t3; };
__device__ __forceinline__ u32 pkw(float f, u32 t) { return ((__builtin_bit_cast(u32, f) + 0x800u) & 0xfffff000u) | t; }
__device__ __forceinline__ v4u pack_g(f32x4 v, const Tag& t) { return v4u{pkw(v[0], t.t0), pkw(v[1], t.t1), pkw(v[2], t.t2), pkw(v[3], t.t3)}; }
__device__ __forceinline__ u32 bad_g(v4u g, const Tag& t) { return ((g[0] ^ t.t0) | (g[1] ^ t.t1) | (g[2] ^ t.t2) | (g[3] ^ t.t3)) & 0xfffu; }
__device__ __forceinline__ f32x4 val_g(v4u g) {
  return f32x4{__builtin_bit_cast(float, g[0] & 0xfffff000u), __builtin_bit_cast(float, g[1] & 0xfffff000u),
               __builtin_bit_cast(float, g[2] & 0xfffff000u), __builtin_bit_cast(float, g[3] & 0xfffff000u)};
}
constexpr int AUX_SC1 = 16;
__device__ __forceinline__ v4u ld_g(__amdgpu_buffer_rsrc_t rs, u32 off) { return __builtin_amdgcn_raw_buffer_load_b128(rs, off, 0, AUX_SC1); }
__device__ __forceinline__ void st_g(__amdgpu_buffer_rsrc_t rs, u32 off, v4u v) { __builtin_amdgcn_raw_buffer_store_b128(v, rs, off, 0, AUX_SC1); }

template <int NT> __device__ __forceinline__ void glds(const char* sbase, u32 voff, u32 dst) {
  if constexpr (NT)
    asm volatile("s_mov_b32 m0, %2\n\ts_nop 0\n\tglobal_load_lds_dwordx4 %0, %1 nt" ::"v"(voff), "s"(sbase), "s"(dst) : "memory");
  else
    asm volatile("s_mov_b32 m0, %2\n\ts_nop 0\n\tglobal_load_lds_dwordx4 %0, %1" ::"v"(voff), "s"(sbase), "s"(dst) : "memory");
}
template <int N> __device__ __forceinline__ void waitvm() { asm volatile("s_waitcnt vmcnt(%0)" ::"i"(N) : "memory"); }
__device__ __forceinline__ void wg_barrier() { asm volatile("s_waitcnt lgkmcnt(0)\n\ts_barrier" ::: "memory"); }

__device__ __forceinline__ f32x4 mfma(v4u a, v4u b, f32x4 c) {
  return __builtin_amdgcn_mfma_f32_16x16x32_bf16(__builtin_bit_cast(s16x8, a), __builtin_bit_cast(s16x8, b), c, 0, 0, 0);
}
__device__ __forceinline__ v2u tr_rd(LAS char* L, u32 a) {
  const s16x4 v = __builtin_amdgcn_ds_read_tr16_b64_v4i16((LAS s16x4*)(L + a));
  return __builtin_bit_cast(v2u, v);
}
__device__ __forceinline__ v4u tr_frag(LAS char* L, u32 a0, u32 a1) {
  const v2u x = tr_rd(L, a0), y = tr_rd(L, a1);
  return v4u{x[0], x[1], y[0], y[1]};
}
__device__ __forceinline__ v4u lds128(LAS char* L, u32 a) { return *(LAS v4u*)(L + a); }

#define STAMP(k) do { } while (0)
#define STAMPS_OUT() do { } while (0)

constexpr u32 C_A = L_FLAG, C_P = L_FLAG + 4, C_Q = L_FLAG + 8, C_M = L_FLAG + 12, C_W2 = L_FLAG + 16;
constexpr u32 F_K = L_FLAG + 32, F_P = L_FLAG + 64;
constexpr u32 L_TAG = L_FLAG + 96;
constexpr u32 L_MX = L_FLAG + 128;
static_assert(L_MX + 64 <= 163840 && L_FLAG % 16 == 0 && L_ML % 16 == 0, "LDS");

__device__ __forceinline__ u32 lds_ld(LAS char* L, u32 a) { return *(volatile LAS u32*)(L + a); }
__device__ __forceinline__ void lds_st(LAS char* L, u32 a, u32 v) { *(volatile LAS u32*)(L + a) = v; }
__device__ __forceinline__ void lds_arrive(LAS char* L, u32 a, int lane) {
  asm volatile("s_waitcnt lgkmcnt(0)" ::: "memory");
  if (lane == 0) __hip_atomic_fetch_add((LAS u32*)(L + a), 1u, __ATOMIC_RELAXED, __HIP_MEMORY_SCOPE_WORKGROUP);
}
__device__ __forceinline__ void lds_flag(LAS char* L, u32 a, int lane) {
  asm volatile("s_waitcnt lgkmcnt(0)" ::: "memory");
  if (lane == 0) lds_st(L, a, 1u);
}
template <int SLEEP> __device__ __forceinline__ void lds_wait(LAS char* L, u32 a, u32 n) {
  while (lds_ld(L, a) < n) { if (SLEEP) __builtin_amdgcn_s_sleep(SLEEP); }
  asm volatile("" ::: "memory");
}
__device__ __forceinline__ Tag make_tag(u64 e) {
  Tag t; t.t0 = (u32)e & 0xfffu; t.t1 = (u32)(e >> 12) & 0xfffu; t.t2 = t.t0; t.t3 = (u32)(e >> 24) & 0xfffu; return t;
}
__device__ __forceinline__ u32 chk(u32 acc, v4u g, const Tag& t) {
  acc = __builtin_amdgcn_bitop3_b32(acc, g[0], t.t0, 0xF6);
  acc = __builtin_amdgcn_bitop3_b32(acc, g[1], t.t1, 0xF6);
  acc = __builtin_amdgcn_bitop3_b32(acc, g[2], t.t2, 0xF6);
  acc = __builtin_amdgcn_bitop3_b32(acc, g[3], t.t3, 0xF6);
  return acc;
}
template <int N, int SLEEP> __device__ __forceinline__ u32 gather(__amdgpu_buffer_rsrc_t rs, const u32 (&off)[N], v4u (&g)[N], const Tag& tg) {
  u32 spin = 0;
  while (true) {
#pragma unroll
    for (int i = 0; i < N; ++i) g[i] = ld_g(rs, off[i]);
    u32 acc0 = 0, acc1 = 0;
#pragma unroll
    for (int i = 0; i < N; ++i) { if (i & 1) acc1 = chk(acc1, g[i], tg); else acc0 = chk(acc0, g[i], tg); }
    if (__all(((acc0 | acc1) & 0xfffu) == 0u) || ++spin > SPIN_CAP) break;
    if (SLEEP) __builtin_amdgcn_s_sleep(SLEEP);
    asm volatile("" ::: "memory");
  }
  return spin;
}
#define SPINS(k, e) do { (void)(e); } while (0)

__global__ void __launch_bounds__(832, 1) fused_decode_kernel(Params p) {
  __shared__ __attribute__((aligned(16))) char lds_raw[LDS_BYTES];
  LAS char* L = (LAS char*)lds_raw;
  const u32 lbase = (u32)(uintptr_t)L;
  const int tid = threadIdx.x, lane = tid & 63;
  const int wave = __builtin_amdgcn_readfirstlane(tid >> 6);
  const int wg = blockIdx.x, xx = (wg + ROT) & 7, j = wg >> 3;
  const int g = lane >> 4;
  asm volatile("" ::"s"(p.x), "s"(p.kc), "s"(p.vc), "s"(p.wq), "s"(p.wo), "s"(p.w1), "s"(p.w2), "s"(p.out), "s"(p.ws));
  STAMP(0);
  const int cs = j >> 2, ks = j & 3;
  const int hh = 4 * xx + (j >> 3), ns = j & 7;
  const int bb = j >> 2, sp = j & 3;
  const bool ffn = xx < 7;
  const u32 trq = (u32)((lane >> 2) & 3), trp = (u32)(lane & 3);
  const u32 trf = ((trq >> 1) & 1) | (((u32)g & 1) << 1);
  const u32 tr_row = ((u32)g * 8 + trq) * 128 + trp * 8;

  if (wave == 12) {
    if (j == 0) asm volatile("buffer_wbl2 sc1\n\ts_waitcnt vmcnt(0)" ::: "memory");
    __builtin_amdgcn_endpgm();
    __builtin_unreachable();
  }
  if (wave >= 4) {
    __builtin_amdgcn_s_setprio(LPRIO);
    const int w = wave - 4;
    const u32 wchunk = (u32)((lane & 7) ^ (((lane >> 4) & 1) << 1));
    const u32 wrow = (u32)(lane >> 3);
    {
      const char* b0 = p.wq + ((size_t)(ks * 128 + 16 * w) * 4096 + xx * 512 + cs * 64) * 2;
      const u32 vo = wrow * 8192 + wchunk * 16;
      glds<W_NT>(b0, vo, lbase + L_W + (2 * w) * 1024);
      glds<W_NT>(b0 + 8 * 8192, vo ^ 64u, lbase + L_W + (2 * w + 1) * 1024);
      if (ffn) {
        const char* b1 = p.w1 + ((size_t)(ns * 64 + 8 * w) * FF + hh * 64) * 2;
        const u32 vo1 = wrow * (FF * 2) + ((wchunk ^ ((u32)(w & 1) << 2)) * 16);
        glds<W_NT>(b1, vo1, lbase + L_W1 + w * 1024);
      }
    }
    wg_barrier();
    const size_t kvoff = ((((size_t)bb * HKV + xx) * SK + (SK - WINDOW) + sp * 256 + w * 32) * DH) * 2;
    const char* kb = p.kc + kvoff;
    const char* vb = p.vc + kvoff;
    const u32 r4 = (u32)(lane >> 4), c16 = (u32)(lane & 15);
    const u32 kvo = r4 * 256 + ((c16 ^ r4) * 16);
    const u32 vvo = r4 * 256 + ((c16 ^ (r4 << 1)) * 16);
    auto issueK = [&](int u) { const int pu = (u + j) & 7; glds<KV_NT>(kb + pu * 1024, kvo ^ (u32)((pu & 3) << 6), lbase + L_K + w * 8192 + pu * 1024); };
    auto issueV = [&](int u) { const int pu = (u + j) & 7; glds<KV_NT>(vb + pu * 1024, vvo ^ (u32)((pu & 1) << 7), lbase + L_V + w * 8192 + pu * 1024); };
    issueK(0); issueK(1); issueK(2);
    waitvm<3>();
    lds_arrive(L, C_A, lane);
    STAMP(1);
    issueK(3); waitvm<WIN>();
    issueK(4); waitvm<WIN>();
    issueK(5); waitvm<WIN>();
    issueK(6); waitvm<WIN>();
    issueK(7); waitvm<WIN>();
    issueV(0); waitvm<WIN>();
    issueV(1); waitvm<2>();
    lds_flag(L, F_K + w * 4, lane);
    STAMP(2);
    issueV(2); waitvm<WIN>();
    issueV(3); waitvm<WIN>();
    issueV(4); waitvm<WIN>();
    issueV(5); waitvm<WIN>();
    issueV(6); waitvm<WIN>();
    issueV(7); waitvm<WIN>();
    lds_wait<1>(L, C_P, 4);
    {
      const char* b0 = p.wo + ((size_t)(hh * 128 + 16 * w) * DM + ns * 64) * 2;
      const u32 vo = wrow * 1024 + wchunk * 16;
      glds<W_NT>(b0, vo, lbase + L_W + (2 * w) * 1024); waitvm<WIN>();
      glds<W_NT>(b0 + 8 * 1024, vo ^ 64u, lbase + L_W + (2 * w + 1) * 1024);
      if (ffn) {
        const char* b1 = p.w2 + ((size_t)(hh * 64 + 8 * w) * DM + ns * 64) * 2;
        glds<W_NT>(b1, wrow * 1024 + ((wchunk ^ ((u32)(w & 1) << 2)) * 16), lbase + L_W1 + w * 1024);
      }
    }
    STAMP(3);
    lds_wait<1>(L, F_P + w * 4, 1);
    const v4u P = lds128(L, L_K + w * 8192 + 4096 + g * 64 + (lane & 3) * 16);
    const u32 vrow = (u32)g * 4 + trq;
    const u32 va = L_V + w * 8192 + vrow * 256 + ((vrow & 7) * 32) + trp * 8;
    STAMP(4);
    if (ffn) waitvm<3>(); else waitvm<2>();
    STAMP(5);
    {
      v4u A[8];
#pragma unroll
      for (int dt = 0; dt < 8; ++dt) { const u32 a = va ^ (u32)(dt << 5); A[dt] = tr_frag(L, a, a + 4096); }
      f32x4 o[8];
#pragma unroll
      for (int dt = 0; dt < 8; ++dt) o[dt] = mfma(A[dt], P, f32x4{0.f, 0.f, 0.f, 0.f});
      if ((lane & 15) < 4) {
#pragma unroll
        for (int dt = 0; dt < 8; ++dt) *(LAS f32x4*)(L + L_K + w * 8192 + (lane & 3) * 512 + dt * 64 + g * 16) = o[dt];
      }
    }
    wg_barrier();
    STAMP(6);
    waitvm<0>();
    lds_arrive(L, C_W2, lane);
    if (w < 4) {
      const __amdgpu_buffer_rsrc_t rs = __builtin_amdgcn_make_buffer_rsrc((void*)p.ws, 0, (int)WS_END, 0x00020000);
      const v4u tv = lds128(L, L_TAG);
      const Tag tg{tv[0], tv[1], tv[2], tv[3]};
      lds_wait<1>(L, C_W2, 8);
      STAMP(7);
      const int nt = w;
      const u32 a0 = tr_row + (((u32)nt ^ trf) * 32);
      v4u A[4];
#pragma unroll
      for (int t = 0; t < 4; ++t) A[t] = tr_frag(L, L_W + a0 + t * 4096, L_W + a0 + t * 4096 + 512);
      f32x4 acc = {0.f, 0.f, 0.f, 0.f};
      if (ffn) {
#pragma unroll
        for (int t = 0; t < 2; ++t) {
          const v4u A2 = tr_frag(L, L_W1 + a0 + t * 4096, L_W1 + a0 + t * 4096 + 512);
          const v4u B = lds128(L, L_XH + (lane & 7) * 160 + (4 * t + g) * 16);
          acc = mfma(B, A2, acc);
        }
      }
      wg_barrier();
      STAMP(8);
#pragma unroll
      for (int t = 0; t < 4; ++t) {
        const v4u B = lds128(L, L_XQ + (lane & 7) * 288 + (4 * t + g) * 16);
        acc = mfma(B, A[t], acc);
      }
      if (lane < 32) st_g(rs, WS_F + ((((ns * 4 + nt) * 2 + g) * 32 + hh) * 16 + (lane & 15)) * 16, pack_g(acc, tg));
      STAMP(9);
    }
    STAMPS_OUT();
    __builtin_amdgcn_s_waitcnt(0);
    __builtin_amdgcn_endpgm();
    __builtin_unreachable();
  } else {
    const int w = wave;
    const __amdgpu_buffer_rsrc_t rs = __builtin_amdgcn_make_buffer_rsrc((void*)p.ws, 0, (int)WS_END, 0x00020000);
    const int fnt = hh & 3, fG = (hh >> 2) & 1, fcq = hh >> 3;
    const int fb = 4 * fG + 2 * (lane >> 5) + ((lane >> 4) & 1);
    const int fcol = ns * 64 + fnt * 16 + 4 * fcq + (lane & 3);
    v4u xv[2];
    v2u ev;
    u32 xres;
    {
      const u32 xo = (u32)(2 * w * 1024 + lane * 16), zero = 0, ro = (u32)(fb * DM + fcol) * 2;
      asm volatile("global_load_dwordx4 %0, %1, %2" : "=v"(xv[0]) : "v"(xo), "s"(p.x) : "memory");
      asm volatile("global_load_dwordx4 %0, %1, %2 offset:1024" : "=v"(xv[1]) : "v"(xo), "s"(p.x) : "memory");
      asm volatile("global_load_dwordx2 %0, %1, %2 sc1" : "=v"(ev) : "v"(zero), "s"(p.ws) : "memory");
      asm volatile("global_load_ushort %0, %1, %2" : "=v"(xres) : "v"(ro), "s"(p.x) : "memory");
    }
    if (w == 3 && lane < 32) lds_st(L, L_FLAG + lane * 4, 0u);
    wg_barrier();
    asm volatile("s_waitcnt vmcnt(2)" : "+v"(xv[0]), "+v"(xv[1])::"memory");
    STAMP(11);
    {
      float v[2][8], s1[2], s2[2], piv[2];
#pragma unroll
      for (int r = 0; r < 2; ++r) {
#pragma unroll
        for (int q = 0; q < 4; ++q) { v[r][2 * q] = bflo(xv[r][q]); v[r][2 * q + 1] = bfhi(xv[r][q]); }
        piv[r] = u2f(__builtin_amdgcn_readfirstlane(f2u(v[r][0])));
        float d[8];
#pragma unroll
        for (int q = 0; q < 8; ++q) d[q] = v[r][q] - piv[r];
        s1[r] = ((d[0] + d[1]) + (d[2] + d[3])) + ((d[4] + d[5]) + (d[6] + d[7]));
        s2[r] = fmaf(d[1], d[1], d[0] * d[0]) + fmaf(d[3], d[3], d[2] * d[2]) + (fmaf(d[5], d[5], d[4] * d[4]) + fmaf(d[7], d[7], d[6] * d[6]));
      }
#define RED4(C, RM) { const float a = dppz<C, RM>(s1[0]), b = dppz<C, RM>(s2[0]), c = dppz<C, RM>(s1[1]), d = dppz<C, RM>(s2[1]); s1[0] += a; s2[0] += b; s1[1] += c; s2[1] += d; }
      RED4(0x111, 0xf) RED4(0x112, 0xf) RED4(0x114, 0xf) RED4(0x118, 0xf) RED4(0x142, 0xa) RED4(0x143, 0xc)
#undef RED4
#pragma unroll
      for (int r = 0; r < 2; ++r) {
        const float t1 = u2f(__builtin_amdgcn_readlane(f2u(s1[r]), 63)), t2 = u2f(__builtin_amdgcn_readlane(f2u(s2[r]), 63));
        const float md = t1 * (1.f / DM);
        const float var = rbf(fmaxf(fmaf(-md, md, t2 * (1.f / DM)), 0.f));
        const float mean = rbf(piv[r] + md);
        const float den = rbf(__builtin_amdgcn_sqrtf(rbf(var + 1e-5f)));
        const float rd = __builtin_amdgcn_rcpf(den);
        v4u xn;
#pragma unroll
        for (int q = 0; q < 4; ++q) {
          const u32 d2 = pk2(v[r][2 * q] - mean, v[r][2 * q + 1] - mean);
          xn[q] = pk2(bflo(d2) * rd, bfhi(d2) * rd);
        }
        if ((lane >> 4) == ks) *(LAS v4u*)(L + L_XQ + (2 * w + r) * 288 + (lane & 15) * 16) = xn;
        if ((lane >> 3) == ns) *(LAS v4u*)(L + L_XH + (2 * w + r) * 160 + (lane & 7) * 16) = xn;
      }
    }
    asm volatile("s_waitcnt vmcnt(0)" : "+v"(ev), "+v"(xres)::"memory");
    const u64 e = (((u64)(u32)__builtin_amdgcn_readfirstlane(ev[1]) << 32) | (u32)__builtin_amdgcn_readfirstlane(ev[0])) + 1;
    const Tag tg = make_tag(e);
    if (w == 0 && lane == 0) *(LAS v4u*)(L + L_TAG) = v4u{tg.t0, tg.t1, tg.t2, tg.t3};
    lds_arrive(L, C_A, lane);
    STAMP(1);
    lds_wait<1>(L, C_A, 12);
    STAMP(2);
    {
      const u32 a0 = tr_row + (((u32)w ^ trf) * 32);
      v4u A[4], B[4], A2[2], B2[2];
#pragma unroll
      for (int t = 0; t < 4; ++t) { A[t] = tr_frag(L, L_W + a0 + t * 4096, L_W + a0 + t * 4096 + 512); B[t] = lds128(L, L_XQ + (lane & 7) * 288 + (4 * t + g) * 16); }
      if (ffn) {
#pragma unroll
        for (int t = 0; t < 2; ++t) { A2[t] = tr_frag(L, L_W1 + a0 + t * 4096, L_W1 + a0 + t * 4096 + 512); B2[t] = lds128(L, L_XH + (lane & 7) * 160 + (4 * t + g) * 16); }
      }
      f32x4 acc = {0.f, 0.f, 0.f, 0.f}, acc2 = {0.f, 0.f, 0.f, 0.f};
#pragma unroll
      for (int t = 0; t < 4; ++t) acc = mfma(A[t], B[t], acc);
      if (ffn) {
#pragma unroll
        for (int t = 0; t < 2; ++t) acc2 = mfma(A2[t], B2[t], acc2);
      }
      LAS char* T = L + L_TR + w * 1024;
      if ((lane & 15) < 8) { *(LAS f32x4*)(T + (lane & 7) * 64 + g * 16) = acc; *(LAS f32x4*)(T + 512 + (lane & 7) * 64 + g * 16) = acc2; }
      const f32x4 tq = *(LAS f32x4*)(T + lane * 16);
      const int tb = (lane >> 2) & 7, tG = lane & 3;
      const u32 oq = WS_Q + (((xx * 8 + tb) * 4 + ks) * 128 + cs * 16 + w * 4 + tG) * 16;
      const u32 oh = WS_H + (((hh * 8 + ns) * 8 + tb) * 16 + w * 4 + tG) * 16;
      if (lane < 32 || ffn) st_g(rs, lane < 32 ? oq : oh, pack_g(tq, tg));
    }
    lds_arrive(L, C_P, lane);
    STAMP(3);
    if (w < 2) {
      __builtin_amdgcn_s_sleep(GSLEEP0);
      const u32 qo = WS_Q + ((xx * 8 + bb) * 4 * 128 + w * 64 + lane) * 16;
      const u32 off[4] = {qo, qo + 2048, qo + 4096, qo + 6144};
      v4u ga[4];
      SPINS(12, (gather<4, GSLEEP>(rs, off, ga, tg)));
      const f32x4 a = (val_g(ga[0]) + val_g(ga[1])) + (val_g(ga[2]) + val_g(ga[3]));
      *(LAS v2u*)(L + L_QI + (2 * w + (lane >> 5)) * 288 + (lane & 31) * 8) = v2u{pk2(a[0], a[1]), pk2(a[2], a[3])};
      lds_arrive(L, C_Q, lane);
    } else if (ffn) {
      lds_wait<1>(L, C_P, 4);
      __builtin_amdgcn_s_sleep(GSLEEP0);
      const u32 ho = WS_H + (hh * 8 * 128 + (w - 2) * 64 + lane) * 16;
      u32 off[8];
#pragma unroll
      for (int s = 0; s < 8; ++s) off[s] = ho + s * 2048;
      v4u ga[8];
      SPINS(12, (gather<8, GSLEEP>(rs, off, ga, tg)));
      f32x4 a = ((val_g(ga[0]) + val_g(ga[1])) + (val_g(ga[2]) + val_g(ga[3]))) + ((val_g(ga[4]) + val_g(ga[5])) + (val_g(ga[6]) + val_g(ga[7])));
      float r[4];
#pragma unroll
      for (int q = 0; q < 4; ++q) { const float hv = rbf(a[q]); r[q] = hv * __builtin_amdgcn_rcpf(1.f + __builtin_amdgcn_exp2f(-hv * 1.4426950408889634f)); }
      *(LAS v2u*)(L + L_XH + (4 * (w - 2) + (lane >> 4)) * 160 + (lane & 15) * 8) = v2u{pk2(r[0], r[1]), pk2(r[2], r[3])};
    }
    STAMP(4);
    lds_wait<1>(L, C_Q, 2);
    lds_wait<1>(L, F_K + (2 * w) * 4, 1);
    lds_wait<1>(L, F_K + (2 * w + 1) * 4, 1);
    STAMP(5);
    {
      const u32 c16 = (u32)(lane & 15);
      float s[2][8], mw[2];
      v4u B[4];
#pragma unroll
      for (int t = 0; t < 4; ++t) B[t] = lds128(L, L_QI + (lane & 3) * 288 + (4 * t + g) * 16);
#pragma unroll
      for (int u = 0; u < 2; ++u) {
        f32x4 s0 = {0.f, 0.f, 0.f, 0.f}, s1 = {0.f, 0.f, 0.f, 0.f};
        const u32 ka = L_K + (2 * w + u) * 8192 + c16 * 256 + (((u32)g ^ c16) * 16);
        v4u A0[4], A1[4];
#pragma unroll
        for (int t = 0; t < 4; ++t) { A0[t] = lds128(L, ka ^ (u32)(t << 6)); A1[t] = lds128(L, (ka ^ (u32)(t << 6)) + 4096); }
#pragma unroll
        for (int t = 0; t < 4; ++t) { s0 = mfma(A0[t], B[t], s0); s1 = mfma(A1[t], B[t], s1); }
#pragma unroll
        for (int r = 0; r < 4; ++r) { s[u][r] = rbf(rbf(s0[r]) * 0.08838834764831845f); s[u][4 + r] = rbf(rbf(s1[r]) * 0.08838834764831845f); }
        mw[u] = fmaxf(fmaxf(fmaxf(s[u][0], s[u][1]), fmaxf(s[u][2], s[u][3])), fmaxf(fmaxf(s[u][4], s[u][5]), fmaxf(s[u][6], s[u][7])));
      }
      const float m = x16_32_max(fmaxf(mw[0], mw[1]));
      if (lane < 4) *(LAS float*)(L + L_MX + (lane * 4 + w) * 4) = m;
      lds_arrive(L, C_M, lane);
      lds_wait<1>(L, C_M, 4);
      const f32x4 m0 = *(LAS f32x4*)(L + L_MX + (lane & 3) * 16);
      const float M = fmaxf(fmaxf(m0[0], m0[1]), fmaxf(m0[2], m0[3]));
      float l = 0.f;
#pragma unroll
      for (int u = 0; u < 2; ++u) {
        float ex[8];
#pragma unroll
        for (int r = 0; r < 8; ++r) { ex[r] = __builtin_amdgcn_exp2f((s[u][r] - M) * 1.4426950408889634f); l += ex[r]; }
        if ((lane & 15) < 4) *(LAS v4u*)(L + L_K + (2 * w + u) * 8192 + 4096 + g * 64 + (lane & 3) * 16) = v4u{pk2(ex[0], ex[1]), pk2(ex[2], ex[3]), pk2(ex[4], ex[5]), pk2(ex[6], ex[7])};
      }
      l = x16_32_sum(l);
      if (lane < 4) { *(LAS float*)(L + L_ML + (lane * 4 + w) * 4) = l; if (w == 0) *(LAS float*)(L + L_ML + 128 + lane * 4) = M; }
      asm volatile("s_waitcnt lgkmcnt(0)" ::: "memory");
      if (lane < 2) lds_st(L, F_P + (2 * w + lane) * 4, 1u);
    }
    STAMP(6);
    wg_barrier();
    STAMP(7);
    {
      const int dq = lane & 31;
      f32x4 o[8];
#pragma unroll
      for (int u = 0; u < 8; ++u) o[u] = *(LAS f32x4*)(L + L_K + u * 8192 + w * 512 + dq * 16);
      const f32x4 l0 = *(LAS f32x4*)(L + L_ML + w * 16);
      const float M = *(LAS float*)(L + L_ML + 128 + w * 4);
      f32x4 O = ((o[0] + o[1]) + (o[2] + o[3])) + ((o[4] + o[5]) + (o[6] + o[7]));
      if (lane >= 32) O = f32x4{M, (l0[0] + l0[1]) + (l0[2] + l0[3]), 0.f, 0.f};
      const int rec = ((4 * xx + w) * 8 + bb) * 4 + sp;
      if (lane < 33) st_g(rs, lane < 32 ? WS_O + (rec * 32 + lane) * 16 : WS_ML + rec * 16, pack_g(O, tg));
    }
    STAMP(8);
    {
      const int b = lane >> 3, i = lane & 7;
      const u32 oo = WS_O + ((hh * 8 + b) * 4 * 32 + 8 * w + i) * 16;
      const u32 off[5] = {WS_ML + (hh * 32 + b * 4 + (lane & 3)) * 16, oo, oo + 512, oo + 1024, oo + 1536};
      v4u gg[5];
      __builtin_amdgcn_s_sleep(HSLEEP0);
      SPINS(13, (gather<5, HSLEEP>(rs, off, gg, tg)));
      STAMP(9);
      const float ms = u2f(gg[0][0] & 0xfffff000u), ls = u2f(gg[0][1] & 0xfffff000u);
      float M = fmaxf(ms, dppm<0xB1>(ms));
      M = fmaxf(M, dppm<0x4E>(M));
      const float ew = __builtin_amdgcn_exp2f((ms - M) * 1.4426950408889634f);
      float Ls = ew * ls;
      Ls += dppm<0xB1>(Ls);
      Ls += dppm<0x4E>(Ls);
      const float wn = ew * __builtin_amdgcn_rcpf(Ls);
      const f32x4 O = (val_g(gg[1]) * dppm<0x00>(wn) + val_g(gg[2]) * dppm<0x55>(wn)) + (val_g(gg[3]) * dppm<0xAA>(wn) + val_g(gg[4]) * dppm<0xFF>(wn));
      *(LAS v2u*)(L + L_XQ + b * 288 + (32 * w + 4 * i) * 2) = v2u{pk2(O[0], O[1]), pk2(O[2], O[3])};
      wg_barrier();
    }
    if (w == 0) {
      const u32 fo = WS_F + ((((ns * 4 + fnt) * 2 + fG) * 32 + (lane >> 2)) * 16 + 4 * fcq + (lane & 3)) * 16;
      const u32 off[2] = {fo, fo + 16 * 256};
      v4u gg[2];
      __builtin_amdgcn_s_sleep(FSLEEP0);
      SPINS(14, (gather<2, FSLEEP>(rs, off, gg, tg)));
      STAMP(10);
      const f32x4 v = val_g(gg[0]) + val_g(gg[1]);
      auto a02 = __builtin_amdgcn_permlane32_swap(f2u(v[0]), f2u(v[2]), false, false);
      auto a13 = __builtin_amdgcn_permlane32_swap(f2u(v[1]), f2u(v[3]), false, false);
      const u32 p0 = a02[0], p1 = a02[1], p2 = a13[0], p3 = a13[1];
      const float lo = u2f(p0) + u2f(p1), hi = u2f(p2) + u2f(p3);
      auto a = __builtin_amdgcn_permlane16_swap(f2u(lo), f2u(hi), false, false);
      const u32 q0 = a[0], q1 = a[1];
      float t = u2f(q0) + u2f(q1);
      t += dppm<0x124>(t);
      t += dppm<0x128>(t);
      if ((lane & 12) == 0) *(GAS unsigned short*)(p.out + (fb * DM + fcol) * 2) = (unsigned short)pk2(bflo(xres) + t, 0.f);
      if (wg == 0 && lane == 0) __hip_atomic_store((GAS u64*)(p.ws + WS_EPOCH), e, __ATOMIC_RELAXED, __HIP_MEMORY_SCOPE_AGENT);
    }
    STAMPS_OUT();
    __builtin_amdgcn_s_waitcnt(0);
    __builtin_amdgcn_endpgm();
    __builtin_unreachable();
  }
}

}

extern "C" void kernel_launch(void* const* d_in, const int* in_sizes, int n_in, void* d_out, int out_size, void* d_ws,
                              size_t ws_size, hipStream_t stream) {
  (void)in_sizes; (void)n_in; (void)out_size; (void)ws_size;
  Params p;
  p.x = (const char*)d_in[0];
  p.kc = (const char*)d_in[1];
  p.vc = (const char*)d_in[2];
  p.wq = (const char*)d_in[3];
  p.wo = (const char*)d_in[4];
  p.w1 = (const char*)d_in[6];
  p.w2 = (const char*)d_in[7];
  p.out = (char*)d_out;
  p.ws = (char*)d_ws;
  hipLaunchKernelGGL(fused_decode_kernel, dim3(NWG), dim3(832), 0, stream, p);
}
```

```cpp
#include <hip/hip_runtime.h>
#include <hip/hip_bf16.h>
#include <stdint.h>

constexpr int B = 8, D = 512, HQ = 32, HKV = 8, SK = 4096, DH = 128, W = 1024, F = 1792;
constexpr int NSPLIT = 4, KPS = W / NSPLIT;

__device__ __forceinline__ float bf2f(unsigned short b) { return __builtin_bit_cast(float, (unsigned)b << 16); }
__device__ __forceinline__ unsigned short f2bf(float f) {
  __hip_bfloat16 h = __float2bfloat16(f); return __builtin_bit_cast(unsigned short, h);
}
__device__ __forceinline__ float rbf(float f) { return bf2f(f2bf(f)); }


__device__ __forceinline__ unsigned long long rtc() { unsigned long long t; asm volatile("s_memrealtime %0\n\ts_waitcnt lgkmcnt(0)" : "=s"(t)::"memory"); return t; }
#define PROBE_BEGIN unsigned long long t0_ = rtc();
#define PROBE_END { unsigned long long t1_ = rtc(); unsigned long long until_ = t1_ + 10 * (t1_ - t0_); while (rtc() < until_) __builtin_amdgcn_s_sleep(4); }
constexpr size_t WS_Q = 0;
constexpr size_t WS_H = WS_Q + B * 4096;
constexpr size_t WS_PO = WS_H + B * F;
constexpr size_t WS_ML = WS_PO + (size_t)B * HQ * NSPLIT * DH;

__device__ void layernorm_all(const unsigned short* x, float* xn  , int tid, int nthr) {
  int wave = tid >> 6, lane = tid & 63;
  for (int b = wave; b < B; b += nthr / 64) {
    float v[8]; float s = 0.f;
    for (int j = 0; j < 8; ++j) { v[j] = bf2f(x[b * D + lane * 8 + j]); s += v[j]; }
    for (int o = 32; o; o >>= 1) s += __shfl_xor(s, o);
    float mean = s / D;
    float ss = 0.f;
    for (int j = 0; j < 8; ++j) { float d = v[j] - mean; ss += d * d; }
    for (int o = 32; o; o >>= 1) ss += __shfl_xor(ss, o);
    float var = rbf(ss / D);
    float m = rbf(mean);
    float den = rbf(sqrtf(rbf(var + 1e-5f)));
    for (int j = 0; j < 8; ++j) xn[b * D + lane * 8 + j] = rbf(rbf(v[j] - m) / den);
  }
}

__global__ void __launch_bounds__(512) k1(const unsigned short* x, const unsigned short* wq, const unsigned short* w1, float* ws) {
  __shared__ float xn[B * D];
  __shared__ float red[8][B][64];
  int tid = threadIdx.x;
  layernorm_all(x, xn, tid, 512);
  __syncthreads();
  int blk = blockIdx.x; bool isq = blk < 64;
  const unsigned short* w = isq ? wq : w1; int ld = isq ? 4096 : F; int c0 = isq ? blk * 64 : (blk - 64) * 64;
  int wave = tid >> 6, lane = tid & 63;
  float acc[B]; for (int b = 0; b < B; ++b) acc[b] = 0.f;
  for (int k = wave * 64; k < wave * 64 + 64; ++k) {
    float wv = bf2f(w[(size_t)k * ld + c0 + lane]);
    for (int b = 0; b < B; ++b) acc[b] += xn[b * D + k] * wv;
  }
  for (int b = 0; b < B; ++b) red[wave][b][lane] = acc[b];
  __syncthreads();
  { int b = wave; float s = 0.f; for (int w8 = 0; w8 < 8; ++w8) s += red[w8][b][lane];
    s = rbf(s);
    if (isq) ws[WS_Q + b * 4096 + c0 + lane] = s;
    else { float sg = s / (1.f + __expf(-s)); ws[WS_H + b * F + c0 + lane] = rbf(sg); } }
}

__global__ void __launch_bounds__(256) k2(const unsigned short* kc, const unsigned short* vc, float* ws) {
  PROBE_BEGIN

  __shared__ float q[4][DH]; __shared__ float p[4][KPS]; __shared__ float red[4][4];
  int blk = blockIdx.x, s = blk % NSPLIT, kvh = (blk / NSPLIT) % HKV, b = blk / (NSPLIT * HKV);
  int tid = threadIdx.x, wave = tid >> 6, lane = tid & 63;
  for (int i = tid; i < 4 * DH; i += 256) q[i / DH][i % DH] = ws[WS_Q + b * 4096 + kvh * 512 + i];
  __syncthreads();
  const unsigned short* kb = kc + (((size_t)b * HKV + kvh) * SK + (SK - W) + s * KPS) * DH;
  const unsigned short* vb = vc + (((size_t)b * HKV + kvh) * SK + (SK - W) + s * KPS) * DH;
  float sc[4] = {0, 0, 0, 0};
  for (int d = 0; d < DH; d += 8) {
    uint4 u = *(const uint4*)(kb + (size_t)tid * DH + d);
    unsigned uu[4] = {u.x, u.y, u.z, u.w};
    for (int j = 0; j < 4; ++j) { float lo = bf2f(uu[j] & 0xffff), hi = bf2f(uu[j] >> 16);
      for (int g = 0; g < 4; ++g) sc[g] += q[g][d + 2 * j] * lo + q[g][d + 2 * j + 1] * hi; }
  }
  const float scale = rbf(0.08838834764831845f);
  float m[4];
  for (int g = 0; g < 4; ++g) { sc[g] = rbf(rbf(sc[g]) * 0.08838834764831845f); float t = sc[g];
    for (int o = 32; o; o >>= 1) t = fmaxf(t, __shfl_xor(t, o)); if (lane == 0) red[wave][g] = t; }
  (void)scale;
  __syncthreads();
  for (int g = 0; g < 4; ++g) m[g] = fmaxf(fmaxf(red[0][g], red[1][g]), fmaxf(red[2][g], red[3][g]));
  __syncthreads();
  float l[4];
  for (int g = 0; g < 4; ++g) { float e = __expf(sc[g] - m[g]); p[g][tid] = e; float t = e;
    for (int o = 32; o; o >>= 1) t += __shfl_xor(t, o); if (lane == 0) red[wave][g] = t; }
  __syncthreads();
  for (int g = 0; g < 4; ++g) l[g] = red[0][g] + red[1][g] + red[2][g] + red[3][g];
  int d = tid & 127, g0 = (tid >> 7) * 2; float o0 = 0, o1 = 0;
  for (int k = 0; k < KPS; ++k) { float v = bf2f(vb[(size_t)k * DH + d]); o0 += p[g0][k] * v; o1 += p[g0 + 1][k] * v; }
  size_t r0 = ((size_t)b * HQ + kvh * 4 + g0) * NSPLIT + s, r1 = r0 + NSPLIT;
  ws[WS_PO + r0 * DH + d] = o0; ws[WS_PO + r1 * DH + d] = o1;
  if (d == 0) { ws[WS_ML + r0 * 2] = m[g0]; ws[WS_ML + r0 * 2 + 1] = l[g0]; ws[WS_ML + r1 * 2] = m[g0 + 1]; ws[WS_ML + r1 * 2 + 1] = l[g0 + 1]; }
  PROBE_END
}

__global__ void __launch_bounds__(512) k3(const unsigned short* x, const unsigned short* wo, const unsigned short* w2, const float* ws, unsigned short* out) {
  __shared__ float o[4096]; __shared__ float redA[8][64], redB[8][64];
  int b = blockIdx.x / 8, c0 = (blockIdx.x % 8) * 64, tid = threadIdx.x, wave = tid >> 6, lane = tid & 63;
  for (int i = tid; i < 4096; i += 512) { int h = i / DH, d = i % DH; size_t r = ((size_t)b * HQ + h) * NSPLIT;
    float M = -INFINITY; for (int s = 0; s < NSPLIT; ++s) M = fmaxf(M, ws[WS_ML + (r + s) * 2]);
    float L = 0, a = 0; for (int s = 0; s < NSPLIT; ++s) { float wt = __expf(ws[WS_ML + (r + s) * 2] - M); L += wt * ws[WS_ML + (r + s) * 2 + 1]; a += wt * ws[WS_PO + (r + s) * DH + d]; }
    o[i] = rbf(a / L); }
  __syncthreads();
  float a = 0.f, bb = 0.f;
  for (int k = wave * 512; k < wave * 512 + 512; ++k) a += o[k] * bf2f(wo[(size_t)k * D + c0 + lane]);
  for (int k = wave * 224; k < wave * 224 + 224; ++k) bb += ws[WS_H + b * F + k] * bf2f(w2[(size_t)k * D + c0 + lane]);
  redA[wave][lane] = a; redB[wave][lane] = bb;
  __syncthreads();
  if (wave == 0) { float A = 0, Bv = 0; for (int w8 = 0; w8 < 8; ++w8) { A += redA[w8][lane]; Bv += redB[w8][lane]; }
    float xv = bf2f(x[b * D + c0 + lane]);
    out[b * D + c0 + lane] = f2bf(rbf(xv + rbf(A)) + rbf(Bv)); }
}

extern "C" void kernel_launch(void* const* d_in, const int* in_sizes, int n_in,
                              void* d_out, int out_size, void* d_ws, size_t ws_size,
                              hipStream_t stream) {
  const unsigned short* x = (const unsigned short*)d_in[0];
  const unsigned short* kc = (const unsigned short*)d_in[1];
  const unsigned short* vc = (const unsigned short*)d_in[2];
  const unsigned short* wq = (const unsigned short*)d_in[3];
  const unsigned short* wo = (const unsigned short*)d_in[4];
  const unsigned short* w1 = (const unsigned short*)d_in[6];
  const unsigned short* w2 = (const unsigned short*)d_in[7];
  float* ws = (float*)d_ws;
  k1<<<92, 512, 0, stream>>>(x, wq, w1, ws);
  k2<<<B * HKV * NSPLIT, 256, 0, stream>>>(kc, vc, ws);
  k3<<<64, 512, 0, stream>>>(x, wo, w2, ws, (unsigned short*)d_out);
}
```

```cpp
#include <hip/hip_runtime.h>
#include <stdint.h>

#define GAS __attribute__((address_space(1)))
#define LAS __attribute__((address_space(3)))
typedef unsigned u32;
typedef unsigned long long u64;
typedef u32 v4u __attribute__((ext_vector_type(4)));
typedef u32 v2u __attribute__((ext_vector_type(2)));
typedef float f32x4 __attribute__((ext_vector_type(4)));
typedef float f32x2 __attribute__((ext_vector_type(2)));
typedef short s16x8 __attribute__((ext_vector_type(8)));
typedef short s16x4 __attribute__((ext_vector_type(4)));
typedef __bf16 bf16x2 __attribute__((ext_vector_type(2)));

#define KV_NT 1
#define W_NT 1
#define WIN 2
#define GSLEEP 6
#define LPRIO 3
#define FSLEEP0 20
#define FSLEEP 1
#define MSLEEP0 16
#define MSLEEP 1
#define WO_LATE 2
#define OUT_SC1 0

namespace {

constexpr int DM = 512, HKV = 8, SK = 4096, DH = 128, WINDOW = 1024, FF = 1792;
constexpr int NWG = 256, NTHR = 768;

constexpr u32 WS_EPOCH = 0;
constexpr u32 WS_Q = 4096;
constexpr u32 WS_H = WS_Q + 8 * 8 * 4 * 128 * 16;
constexpr u32 WS_O = WS_H + 28 * 8 * 8 * 16 * 16;
constexpr u32 WS_ML = WS_O + 8 * 8 * 4 * 128 * 16;
constexpr u32 WS_F = WS_ML + 8 * 8 * 4 * 4 * 16;
constexpr u32 WS_END = WS_F + 8 * 4 * 2 * 32 * 16 * 16;
constexpr u32 WS_ST = WS_END;

constexpr u32 L_K = 0, L_V = 65536, L_W = 131072, L_W1 = L_W + 16384;
constexpr u32 L_XQ = L_W + 24576;
constexpr u32 L_XH = L_XQ + 8 * 288;
constexpr u32 L_QI = L_XH + 8 * 160;
constexpr u32 L_TR = L_QI + 4 * 288;
constexpr u32 L_ML = L_TR + 2048;
constexpr u32 L_MX = L_ML + 96;
constexpr u32 L_WT = L_MX + 64;
constexpr u32 L_FLAG = L_WT + 512;
constexpr u32 L_END = L_FLAG + 128;
static_assert(L_END <= 163840 && L_ML % 16 == 0 && L_MX % 16 == 0 && L_WT % 16 == 0 && L_FLAG % 16 == 0, "LDS");
constexpr u32 LDS_BYTES = 163840;
constexpr u32 C_A = L_FLAG, C_P = L_FLAG + 4, C_Q = L_FLAG + 8, C_M = L_FLAG + 12, C_W2 = L_FLAG + 16, C_W1 = L_FLAG + 20, C_H = L_FLAG + 24,
              C_PP = L_FLAG + 28, F_G = L_FLAG + 32, F_ML = L_FLAG + 36, C_HI = L_FLAG + 40;
constexpr u32 F_K = L_FLAG + 64;
constexpr u32 L_TAG = L_FLAG + 96;

constexpr u32 SPIN_CAP = 1u << 16;

struct Params {
  const char* x; const char* kc; const char* vc; const char* wq; const char* wo; const char* w1; const char* w2;
  char* out; char* ws;
};

__device__ __forceinline__ float u2f(u32 u) { return __builtin_bit_cast(float, u); }
__device__ __forceinline__ u32 f2u(float f) { return __builtin_bit_cast(u32, f); }
__device__ __forceinline__ float bflo(u32 u) { return u2f(u << 16); }
__device__ __forceinline__ float bfhi(u32 u) { return u2f(u & 0xffff0000u); }
__device__ __forceinline__ u32 pk2(float lo, float hi) {
  f32x2 v = {lo, hi};
  return __builtin_bit_cast(u32, __builtin_convertvector(v, bf16x2));
}
__device__ __forceinline__ float rbf(float f) { return bflo(pk2(f, f) & 0xffffu); }

template <int CTRL, int RM> __device__ __forceinline__ float dppz(float v) {
  return u2f((u32)__builtin_amdgcn_update_dpp(0, (int)f2u(v), CTRL, RM, 0xf, true));
}
template <int CTRL> __device__ __forceinline__ float dppm(float v) {
  return u2f((u32)__builtin_amdgcn_mov_dpp((int)f2u(v), CTRL, 0xf, 0xf, true));
}
__device__ __forceinline__ float x16_32_max(float x) {
  auto s = __builtin_amdgcn_permlane16_swap(f2u(x), f2u(x), false, false);
  const u32 s0 = s[0], s1 = s[1];
  x = fmaxf(u2f(s0), u2f(s1));
  auto t = __builtin_amdgcn_permlane32_swap(f2u(x), f2u(x), false, false);
  const u32 t0 = t[0], t1 = t[1];
  return fmaxf(u2f(t0), u2f(t1));
}
__device__ __forceinline__ float x16_32_sum(float x) {
  auto s = __builtin_amdgcn_permlane16_swap(f2u(x), f2u(x), false, false);
  const u32 s0 = s[0], s1 = s[1];
  x = u2f(s0) + u2f(s1);
  auto t = __builtin_amdgcn_permlane32_swap(f2u(x), f2u(x), false, false);
  const u32 t0 = t[0], t1 = t[1];
  return u2f(t0) + u2f(t1);
}

struct Tag { u32 t0, t1, t2, t3; };
__device__ __forceinline__ Tag make_tag(u64 e) {
  Tag t; t.t0 = (u32)e & 0xfffu; t.t1 = (u32)(e >> 12) & 0xfffu; t.t2 = t.t0; t.t3 = (u32)(e >> 24) & 0xfffu; return t;
}
__device__ __forceinline__ u32 pkw(float f, u32 t) { return ((f2u(f) + 0x800u) & 0xfffff000u) | t; }
__device__ __forceinline__ v4u pack_g(f32x4 v, const Tag& t) { return v4u{pkw(v[0], t.t0), pkw(v[1], t.t1), pkw(v[2], t.t2), pkw(v[3], t.t3)}; }
__device__ __forceinline__ f32x4 val_g(v4u g) {
  return f32x4{u2f(g[0] & 0xfffff000u), u2f(g[1] & 0xfffff000u), u2f(g[2] & 0xfffff000u), u2f(g[3] & 0xfffff000u)};
}
__device__ __forceinline__ u32 chk(u32 acc, v4u g, const Tag& t) {
  acc = __builtin_amdgcn_bitop3_b32(acc, g[0], t.t0, 0xF6);
  acc = __builtin_amdgcn_bitop3_b32(acc, g[1], t.t1, 0xF6);
  acc = __builtin_amdgcn_bitop3_b32(acc, g[2], t.t2, 0xF6);
  acc = __builtin_amdgcn_bitop3_b32(acc, g[3], t.t3, 0xF6);
  return acc;
}
constexpr int AUX_SC1 = 16;
__device__ __forceinline__ v4u ld_g(__amdgpu_buffer_rsrc_t rs, u32 off) { return __builtin_amdgcn_raw_buffer_load_b128(rs, off, 0, AUX_SC1); }
__device__ __forceinline__ void st_g(__amdgpu_buffer_rsrc_t rs, u32 off, v4u v) { __builtin_amdgcn_raw_buffer_store_b128(v, rs, off, 0, AUX_SC1); }
template <int N, int SLEEP> __device__ __forceinline__ u32 gather(__amdgpu_buffer_rsrc_t rs, const u32 (&off)[N], v4u (&g)[N], const Tag& tg) {
  u32 spin = 0;
  while (true) {
#pragma unroll
    for (int i = 0; i < N; ++i) g[i] = ld_g(rs, off[i]);
    u32 acc0 = 0, acc1 = 0;
#pragma unroll
    for (int i = 0; i < N; ++i) { if (i & 1) acc1 = chk(acc1, g[i], tg); else acc0 = chk(acc0, g[i], tg); }
    if (__all(((acc0 | acc1) & 0xfffu) == 0u) || ++spin > SPIN_CAP) break;
    if (SLEEP) __builtin_amdgcn_s_sleep(SLEEP);
    asm volatile("" ::: "memory");
  }
  return spin;
}

template <int NT> __device__ __forceinline__ void glds(const char* sbase, u32 voff, u32 dst) {
  if constexpr (NT)
    asm volatile("s_mov_b32 m0, %2\n\ts_nop 0\n\tglobal_load_lds_dwordx4 %0, %1 nt" ::"v"(voff), "s"(sbase), "s"(dst) : "memory");
  else
    asm volatile("s_mov_b32 m0, %2\n\ts_nop 0\n\tglobal_load_lds_dwordx4 %0, %1" ::"v"(voff), "s"(sbase), "s"(dst) : "memory");
}
template <int N> __device__ __forceinline__ void waitvm() { asm volatile("s_waitcnt vmcnt(%0)" ::"i"(N) : "memory"); }
__device__ __forceinline__ void wg_barrier() { asm volatile("s_waitcnt lgkmcnt(0)\n\ts_barrier" ::: "memory"); }

__device__ __forceinline__ f32x4 mfma(v4u a, v4u b, f32x4 c) {
  return __builtin_amdgcn_mfma_f32_16x16x32_bf16(__builtin_bit_cast(s16x8, a), __builtin_bit_cast(s16x8, b), c, 0, 0, 0);
}
__device__ __forceinline__ v4u tr_frag(LAS char* L, u32 a0, u32 a1) {
  const v2u x = __builtin_bit_cast(v2u, __builtin_amdgcn_ds_read_tr16_b64_v4i16((LAS s16x4*)(L + a0)));
  const v2u y = __builtin_bit_cast(v2u, __builtin_amdgcn_ds_read_tr16_b64_v4i16((LAS s16x4*)(L + a1)));
  return v4u{x[0], x[1], y[0], y[1]};
}
__device__ __forceinline__ v4u lds128(LAS char* L, u32 a) { return *(LAS v4u*)(L + a); }

__device__ __forceinline__ u32 lds_ld(LAS char* L, u32 a) { return *(volatile LAS u32*)(L + a); }
__device__ __forceinline__ void lds_st(LAS char* L, u32 a, u32 v) { *(volatile LAS u32*)(L + a) = v; }
__device__ __forceinline__ void lds_arrive(u32 lbase, u32 a) {
  const u32 addr = lbase + a, one = 1;
  asm volatile("s_waitcnt lgkmcnt(0)\n\ts_mov_b64 exec, 1\n\tds_add_u32 %0, %1\n\ts_mov_b64 exec, -1" ::"v"(addr), "v"(one) : "memory");
}
__device__ __forceinline__ void lds_flag(u32 lbase, u32 a) {
  const u32 addr = lbase + a, one = 1;
  asm volatile("s_waitcnt lgkmcnt(0)\n\ts_mov_b64 exec, 1\n\tds_write_b32 %0, %1\n\ts_mov_b64 exec, -1" ::"v"(addr), "v"(one) : "memory");
}
template <int SLEEP> __device__ __forceinline__ void lds_wait(LAS char* L, u32 a, u32 n) {
  while (lds_ld(L, a) < n) { if (SLEEP) __builtin_amdgcn_s_sleep(SLEEP); }
  asm volatile("" ::: "memory");
}

#define STAMP(k) do { } while (0)
#define STAMPS_OUT() do { } while (0)
#define SPINS(k, e) do { (void)(e); } while (0)

__global__ void __launch_bounds__(NTHR, 1) fused_decode_kernel(Params p) {
  __shared__ __attribute__((aligned(16))) char lds_raw[LDS_BYTES];
  LAS char* L = (LAS char*)lds_raw;
  const u32 lbase = (u32)(uintptr_t)L;
  const int tid = threadIdx.x, lane = tid & 63;
  const int wave = __builtin_amdgcn_readfirstlane(tid >> 6);
  const int wg = blockIdx.x, xx = wg & 7, j = wg >> 3;
  const int g = lane >> 4;
  asm volatile("" ::"s"(p.x), "s"(p.kc), "s"(p.vc), "s"(p.wq), "s"(p.wo), "s"(p.w1), "s"(p.w2), "s"(p.out), "s"(p.ws));
  STAMP(0);
  const int cs = j >> 2, ks = j & 3;
  const int db = j >> 3, ns = j & 7;
  const int hh = 4 * xx + db;
  const int bb = j >> 2, sp = j & 3;
  const bool ffn = xx < 7;
  const u32 trq = (u32)((lane >> 2) & 3), trp = (u32)(lane & 3);
  const u32 trf = ((trq >> 1) & 1) | (((u32)g & 1) << 1);
  const u32 tr_row = ((u32)g * 8 + trq) * 128 + trp * 8;

  if (wave >= 4) {
    __builtin_amdgcn_s_setprio(LPRIO);
    const int w = wave - 4;
    const u32 wchunk = (u32)((lane & 7) ^ (((lane >> 4) & 1) << 1));
    const u32 wrow = (u32)(lane >> 3);
    {
      const char* b0 = p.wq + ((size_t)(ks * 128 + 16 * w) * 4096 + xx * 512 + cs * 64) * 2;
      const u32 vo = wrow * 8192 + wchunk * 16;
      glds<W_NT>(b0, vo, lbase + L_W + (2 * w) * 1024);
      glds<W_NT>(b0 + 8 * 8192, vo ^ 64u, lbase + L_W + (2 * w + 1) * 1024);
    }
    wg_barrier();
    const size_t kvoff = ((((size_t)bb * HKV + xx) * SK + (SK - WINDOW) + sp * 256 + w * 32) * DH) * 2;
    const char* kb = p.kc + kvoff;
    const char* vb = p.vc + kvoff;
    const u32 r4 = (u32)(lane >> 4), c16 = (u32)(lane & 15);
    const u32 kvo = r4 * 256 + ((c16 ^ r4) * 16);
    const u32 vvo = r4 * 256 + ((c16 ^ (r4 << 1)) * 16);
    auto issueK = [&](int u) { const int pu = (u + j) & 7; glds<KV_NT>(kb + pu * 1024, kvo ^ (u32)((pu & 3) << 6), lbase + L_K + w * 8192 + pu * 1024); };
    auto issueV = [&](int u) { const int pu = (u + j) & 7; glds<KV_NT>(vb + pu * 1024, vvo ^ (u32)((pu & 1) << 7), lbase + L_V + w * 8192 + pu * 1024); };
    issueK(0); issueK(1); issueK(2);
    if (ffn) {
      const char* b1 = p.w1 + ((size_t)(ns * 64 + 8 * w) * FF + hh * 64) * 2;
      glds<W_NT>(b1, wrow * (FF * 2) + ((wchunk ^ ((u32)(w & 1) << 2)) * 16), lbase + L_W1 + w * 1024);
      waitvm<4>();
    } else {
      waitvm<3>();
    }
    lds_arrive(lbase, C_A);
    STAMP(1);
    issueK(3); waitvm<WIN>();
    issueK(4); waitvm<2>();
    lds_arrive(lbase, C_W1);
    issueK(5); waitvm<WIN>();
    issueK(6); waitvm<WIN>();
    issueK(7); waitvm<2>();
    if (w == 0) lds_flag(lbase, F_G);
    issueV(0); waitvm<WIN>();
    issueV(1); waitvm<2>();
    lds_flag(lbase, F_K + w * 4);
    STAMP(2);
    issueV(2); waitvm<WIN>();
    issueV(3); waitvm<WIN>();
    issueV(4); waitvm<WIN>();
    issueV(5); waitvm<WIN>();
    issueV(6); waitvm<WIN>();
    issueV(7);
    STAMP(3);
    lds_wait<1>(L, C_PP, 4);
    v4u P[8];
#pragma unroll
    for (int u = 0; u < 8; ++u) P[u] = lds128(L, L_K + u * 8192 + 4096 + g * 64 + (lane & 3) * 16);
    const f32x4 lsum = *(LAS f32x4*)(L + L_ML + (lane & 3) * 16);
    const float mx = *(LAS float*)(L + L_ML + 64 + (lane & 3) * 4);
    const v4u tv = lds128(L, L_TAG);
    const Tag tg{tv[0], tv[1], tv[2], tv[3]};
    const __amdgpu_buffer_rsrc_t rs = __builtin_amdgcn_make_buffer_rsrc((void*)p.ws, 0, (int)WS_END, 0x00020000);
    const u32 vrow = (u32)g * 4 + trq;
    u32 va = L_V + vrow * 256 + (((u32)w ^ (vrow & 7)) * 32) + trp * 8;
    asm volatile("" : "+v"(va));
    const int rec = (xx * 8 + bb) * 4 + sp;
    const bool mlane = w == 0 && g == 0 && (lane >> 2) == 1;
    const u32 ooff = mlane ? WS_ML + (rec * 4 + (lane & 3)) * 16 : WS_O + (rec * 128 + (4 * w + g) * 4 + (lane & 3)) * 16;
    STAMP(4);
    const char* wob = p.wo + ((size_t)((4 * xx + (w >> 1)) * 128 + 32 * db + 16 * (w & 1)) * DM + ns * 64) * 2;
    const char* w2b = p.w2 + ((size_t)((ffn ? hh : 0) * 64 + 8 * w) * DM + ns * 64) * 2;
    const char* wob1 = wob + 8 * 1024;
    u32 wvo = wrow * 1024 + wchunk * 16, wvo1 = wvo ^ 64u, wvo2 = wrow * 1024 + ((wchunk ^ ((u32)(w & 1) << 2)) * 16);
    asm volatile("" : "+v"(wvo), "+v"(wvo1), "+v"(wvo2) : "s"(wob), "s"(wob1), "s"(w2b));
    lds_wait<0>(L, C_P, 4);
    lds_wait<0>(L, C_H, 4);
    waitvm<0>();
    glds<W_NT>(wob, wvo, lbase + L_W + (2 * w) * 1024);
    glds<W_NT>(wob1, wvo1, lbase + L_W + (2 * w + 1) * 1024);
    if (ffn) glds<W_NT>(w2b, wvo2, lbase + L_W1 + w * 1024);
    STAMP(5);
    wg_barrier();
    STAMP(6);
    {
      v4u A[8];
#pragma unroll
      for (int u = 0; u < 8; ++u) A[u] = tr_frag(L, va + u * 8192, va + u * 8192 + 4096);
      __builtin_amdgcn_sched_barrier(0);
      f32x4 o = {0.f, 0.f, 0.f, 0.f};
#pragma unroll
      for (int u = 0; u < 8; ++u) o = mfma(A[u], P[u], o);
      if (mlane) o = f32x4{mx, (lsum[0] + lsum[1]) + (lsum[2] + lsum[3]), 0.f, 0.f};
      if ((lane & 15) < 4 || mlane) st_g(rs, ooff, pack_g(o, tg));
    }
    STAMP(7);
    if (w >= 4) {
      if (ffn) {
        const int gi = 32 * (w - 4) + (lane & 31);
        const u32 ho = WS_H + ((hh * 8 + 4 * (lane >> 5)) * 128 + gi) * 16;
        const u32 off[4] = {ho, ho + 2048, ho + 4096, ho + 6144};
        v4u ga[4];
        SPINS(12, (gather<4, 2>(rs, off, ga, tg)));
        const f32x4 a = (val_g(ga[0]) + val_g(ga[1])) + (val_g(ga[2]) + val_g(ga[3]));
        float r[4];
#pragma unroll
        for (int q = 0; q < 4; ++q) {
          auto sw = __builtin_amdgcn_permlane32_swap(f2u(a[q]), f2u(a[q]), false, false);
          const u32 s0 = sw[0], s1 = sw[1];
          const float hv = rbf(u2f(s0) + u2f(s1));
          r[q] = hv * __builtin_amdgcn_rcpf(1.f + __builtin_amdgcn_exp2f(-hv * 1.4426950408889634f));
        }
        if (lane < 32) *(LAS v2u*)(L + L_XH + (gi >> 4) * 160 + (gi & 15) * 8) = v2u{pk2(r[0], r[1]), pk2(r[2], r[3])};
        lds_arrive(lbase, C_HI);
      }
      waitvm<0>();
      lds_arrive(lbase, C_W2);
      STAMP(8);
    } else {
      waitvm<0>();
      lds_arrive(lbase, C_W2);
      lds_wait<1>(L, C_W2, WO_LATE == 1 ? 4 : 8);
      const int nt = w;
      const u32 a0 = tr_row + (((u32)nt ^ trf) * 32);
      v4u A[4];
#pragma unroll
      for (int t = 0; t < 4; ++t) A[t] = tr_frag(L, L_W + a0 + t * 4096, L_W + a0 + t * 4096 + 512);
      f32x4 acc = {0.f, 0.f, 0.f, 0.f};
      if (ffn) {
        v4u A2[2];
#pragma unroll
        for (int t = 0; t < 2; ++t) A2[t] = tr_frag(L, L_W1 + a0 + t * 4096, L_W1 + a0 + t * 4096 + 512);
        lds_wait<1>(L, C_HI, 4);
#pragma unroll
        for (int t = 0; t < 2; ++t) acc = mfma(lds128(L, L_XH + (lane & 7) * 160 + (4 * t + g) * 16), A2[t], acc);
      }
      u32 foff = WS_F + ((((ns * 4 + nt) * 2 + g) * 32 + hh) * 16 + (lane & 15)) * 16;
      u32 xqa = L_XQ + (lane & 7) * 288 + g * 16;
      asm volatile("" : "+v"(foff), "+v"(xqa));
      wg_barrier();
      STAMP(8);
      v4u B[4];
#pragma unroll
      for (int t = 0; t < 4; ++t) B[t] = lds128(L, xqa + t * 64);
      __builtin_amdgcn_sched_barrier(0);
#pragma unroll
      for (int t = 0; t < 4; ++t) acc = mfma(B[t], A[t], acc);
      if (lane < 32) st_g(rs, foff, pack_g(acc, tg));
      STAMP(9);
    }
    STAMPS_OUT();
    __builtin_amdgcn_s_waitcnt(0);
    __builtin_amdgcn_endpgm();
    __builtin_unreachable();
  } else {
    const int w = wave;
    const __amdgpu_buffer_rsrc_t rs = __builtin_amdgcn_make_buffer_rsrc((void*)p.ws, 0, (int)WS_END, 0x00020000);
    const int fnt = hh & 3, fG = (hh >> 2) & 1, fcq = hh >> 3;
    const int fb = 4 * fG + 2 * (lane >> 5) + ((lane >> 4) & 1);
    const int fcol = ns * 64 + fnt * 16 + 4 * fcq + (lane & 3);
    v4u xv[2];
    v2u ev;
    u32 xres;
    {
      const u32 xo = (u32)(2 * w * 1024 + lane * 16), zero = 0, ro = (u32)(fb * DM + fcol) * 2;
      asm volatile("global_load_dwordx4 %0, %1, %2" : "=&v"(xv[0]) : "v"(xo), "s"(p.x) : "memory");
      asm volatile("global_load_dwordx4 %0, %1, %2 offset:1024" : "=&v"(xv[1]) : "v"(xo), "s"(p.x) : "memory");
      asm volatile("global_load_dwordx2 %0, %1, %2 sc1" : "=&v"(ev) : "v"(zero), "s"(p.ws) : "memory");
      asm volatile("global_load_ushort %0, %1, %2" : "=&v"(xres) : "v"(ro), "s"(p.x) : "memory");
    }
    if (w == 3 && lane < 32) lds_st(L, L_FLAG + lane * 4, 0u);
    wg_barrier();
    asm volatile("s_waitcnt vmcnt(2)" : "+v"(xv[0]), "+v"(xv[1])::"memory");
    STAMP(11);
    {
      float v[2][8], s1[2], s2[2], piv[2];
#pragma unroll
      for (int r = 0; r < 2; ++r) {
#pragma unroll
        for (int q = 0; q < 4; ++q) { v[r][2 * q] = bflo(xv[r][q]); v[r][2 * q + 1] = bfhi(xv[r][q]); }
        piv[r] = u2f(__builtin_amdgcn_readfirstlane(f2u(v[r][0])));
        float d[8];
#pragma unroll
        for (int q = 0; q < 8; ++q) d[q] = v[r][q] - piv[r];
        s1[r] = ((d[0] + d[1]) + (d[2] + d[3])) + ((d[4] + d[5]) + (d[6] + d[7]));
        s2[r] = fmaf(d[1], d[1], d[0] * d[0]) + fmaf(d[3], d[3], d[2] * d[2]) + (fmaf(d[5], d[5], d[4] * d[4]) + fmaf(d[7], d[7], d[6] * d[6]));
      }
#define RED4(C, RM) { const float a = dppz<C, RM>(s1[0]), b = dppz<C, RM>(s2[0]), c = dppz<C, RM>(s1[1]), d = dppz<C, RM>(s2[1]); s1[0] += a; s2[0] += b; s1[1] += c; s2[1] += d; }
      RED4(0x111, 0xf) RED4(0x112, 0xf) RED4(0x114, 0xf) RED4(0x118, 0xf) RED4(0x142, 0xa) RED4(0x143, 0xc)
#undef RED4
#pragma unroll
      for (int r = 0; r < 2; ++r) {
        const float t1 = u2f(__builtin_amdgcn_readlane(f2u(s1[r]), 63)), t2 = u2f(__builtin_amdgcn_readlane(f2u(s2[r]), 63));
        const float md = t1 * (1.f / DM);
        const float var = rbf(fmaxf(fmaf(-md, md, t2 * (1.f / DM)), 0.f));
        const float mean = rbf(piv[r] + md);
        const float den = rbf(__builtin_amdgcn_sqrtf(rbf(var + 1e-5f)));
        const float rd = __builtin_amdgcn_rcpf(den);
        v4u xn;
#pragma unroll
        for (int q = 0; q < 4; ++q) {
          const u32 d2 = pk2(v[r][2 * q] - mean, v[r][2 * q + 1] - mean);
          xn[q] = pk2(bflo(d2) * rd, bfhi(d2) * rd);
        }
        if ((lane >> 4) == ks) *(LAS v4u*)(L + L_XQ + (2 * w + r) * 288 + (lane & 15) * 16) = xn;
        if ((lane >> 3) == ns) *(LAS v4u*)(L + L_XH + (2 * w + r) * 160 + (lane & 7) * 16) = xn;
      }
    }
    asm volatile("s_waitcnt vmcnt(0)" : "+v"(ev), "+v"(xres)::"memory");
    const u64 e = (((u64)(u32)__builtin_amdgcn_readfirstlane(ev[1]) << 32) | (u32)__builtin_amdgcn_readfirstlane(ev[0])) + 1;
    const Tag tg = make_tag(e);
    if (w == 0 && lane == 0) *(LAS v4u*)(L + L_TAG) = v4u{tg.t0, tg.t1, tg.t2, tg.t3};
    lds_arrive(lbase, C_A);
    STAMP(1);
    LAS char* T = L + L_TR + w * 512;
    const int tb = (lane >> 2) & 7, tG = lane & 3;
    const u32 a0 = tr_row + (((u32)w ^ trf) * 32);
    lds_wait<1>(L, C_A, 12);
    STAMP(2);
    {
      v4u A[4], B[4];
#pragma unroll
      for (int t = 0; t < 4; ++t) { A[t] = tr_frag(L, L_W + a0 + t * 4096, L_W + a0 + t * 4096 + 512); B[t] = lds128(L, L_XQ + (lane & 7) * 288 + (4 * t + g) * 16); }
      __builtin_amdgcn_sched_barrier(0);
      f32x4 acc = {0.f, 0.f, 0.f, 0.f};
#pragma unroll
      for (int t = 0; t < 4; ++t) acc = mfma(A[t], B[t], acc);
      if ((lane & 15) < 8) *(LAS f32x4*)(T + (lane & 7) * 64 + g * 16) = acc;
      const f32x4 tq = *(LAS f32x4*)(T + (lane & 31) * 16);
      if (lane < 32) st_g(rs, WS_Q + (((xx * 8 + tb) * 4 + ks) * 128 + cs * 16 + w * 4 + tG) * 16, pack_g(tq, tg));
    }
    lds_arrive(lbase, C_P);
    STAMP(3);
    if (ffn) {
      lds_wait<1>(L, C_W1, 8);
      v4u A[2], B[2];
#pragma unroll
      for (int t = 0; t < 2; ++t) { A[t] = tr_frag(L, L_W1 + a0 + t * 4096, L_W1 + a0 + t * 4096 + 512); B[t] = lds128(L, L_XH + (lane & 7) * 160 + (4 * t + g) * 16); }
      f32x4 acc = {0.f, 0.f, 0.f, 0.f};
#pragma unroll
      for (int t = 0; t < 2; ++t) acc = mfma(A[t], B[t], acc);
      if ((lane & 15) < 8) *(LAS f32x4*)(T + (lane & 7) * 64 + g * 16) = acc;
      const f32x4 th = *(LAS f32x4*)(T + (lane & 31) * 16);
      if (lane < 32) st_g(rs, WS_H + (((hh * 8 + ns) * 8 + tb) * 16 + w * 4 + tG) * 16, pack_g(th, tg));
    }
    lds_arrive(lbase, C_H);
    if (w < 2) {
      lds_wait<2>(L, F_G, 1);
      const u32 qo = WS_Q + ((xx * 8 + bb) * 4 * 128 + w * 64 + lane) * 16;
      const u32 off[4] = {qo, qo + 2048, qo + 4096, qo + 6144};
      v4u ga[4];
      SPINS(12, (gather<4, GSLEEP>(rs, off, ga, tg)));
      const f32x4 a = (val_g(ga[0]) + val_g(ga[1])) + (val_g(ga[2]) + val_g(ga[3]));
      *(LAS v2u*)(L + L_QI + (2 * w + (lane >> 5)) * 288 + (lane & 31) * 8) = v2u{pk2(a[0], a[1]), pk2(a[2], a[3])};
      lds_arrive(lbase, C_Q);
    }
    STAMP(4);
    lds_wait<1>(L, C_Q, 2);
    lds_wait<1>(L, F_K + (2 * w) * 4, 1);
    lds_wait<1>(L, F_K + (2 * w + 1) * 4, 1);
    STAMP(5);
    {
      const u32 c16 = (u32)(lane & 15);
      v4u B[4], A0[2][4], A1[2][4];
#pragma unroll
      for (int t = 0; t < 4; ++t) B[t] = lds128(L, L_QI + (lane & 3) * 288 + (4 * t + g) * 16);
#pragma unroll
      for (int u = 0; u < 2; ++u) {
        const u32 ka = L_K + (2 * w + u) * 8192 + c16 * 256 + (((u32)g ^ c16) * 16);
#pragma unroll
        for (int t = 0; t < 4; ++t) { A0[u][t] = lds128(L, ka ^ (u32)(t << 6)); A1[u][t] = lds128(L, (ka ^ (u32)(t << 6)) + 4096); }
      }
      __builtin_amdgcn_sched_barrier(0);
      f32x4 sc[4];
#pragma unroll
      for (int u = 0; u < 4; ++u) sc[u] = f32x4{0.f, 0.f, 0.f, 0.f};
#pragma unroll
      for (int t = 0; t < 4; ++t) {
        sc[0] = mfma(A0[0][t], B[t], sc[0]); sc[1] = mfma(A1[0][t], B[t], sc[1]);
        sc[2] = mfma(A0[1][t], B[t], sc[2]); sc[3] = mfma(A1[1][t], B[t], sc[3]);
      }
      float s[16];
#pragma unroll
      for (int u = 0; u < 4; ++u) {
        const u32 a = pk2(sc[u][0], sc[u][1]), b = pk2(sc[u][2], sc[u][3]);
        const u32 c = pk2(bflo(a) * 0.08838834764831845f, bfhi(a) * 0.08838834764831845f);
        const u32 d = pk2(bflo(b) * 0.08838834764831845f, bfhi(b) * 0.08838834764831845f);
        s[4 * u] = bflo(c); s[4 * u + 1] = bfhi(c); s[4 * u + 2] = bflo(d); s[4 * u + 3] = bfhi(d);
      }
      float m = fmaxf(fmaxf(fmaxf(fmaxf(s[0], s[1]), fmaxf(s[2], s[3])), fmaxf(fmaxf(s[4], s[5]), fmaxf(s[6], s[7]))),
                      fmaxf(fmaxf(fmaxf(s[8], s[9]), fmaxf(s[10], s[11])), fmaxf(fmaxf(s[12], s[13]), fmaxf(s[14], s[15]))));
      m = x16_32_max(m);
      if (lane < 4) *(LAS float*)(L + L_MX + (lane * 4 + w) * 4) = m;
      lds_arrive(lbase, C_M);
      lds_wait<0>(L, C_M, 4);
      const f32x4 m0 = *(LAS f32x4*)(L + L_MX + (lane & 3) * 16);
      const float M = fmaxf(fmaxf(m0[0], m0[1]), fmaxf(m0[2], m0[3]));
      const float Ml = M * 1.4426950408889634f;
      float ex[16];
#pragma unroll
      for (int r = 0; r < 16; ++r) ex[r] = __builtin_amdgcn_exp2f(fmaf(s[r], 1.4426950408889634f, -Ml));
      float l = (((ex[0] + ex[1]) + (ex[2] + ex[3])) + ((ex[4] + ex[5]) + (ex[6] + ex[7]))) + (((ex[8] + ex[9]) + (ex[10] + ex[11])) + ((ex[12] + ex[13]) + (ex[14] + ex[15])));
      if ((lane & 15) < 4) {
#pragma unroll
        for (int u = 0; u < 2; ++u)
          *(LAS v4u*)(L + L_K + (2 * w + u) * 8192 + 4096 + g * 64 + (lane & 3) * 16) =
              v4u{pk2(ex[8 * u], ex[8 * u + 1]), pk2(ex[8 * u + 2], ex[8 * u + 3]), pk2(ex[8 * u + 4], ex[8 * u + 5]), pk2(ex[8 * u + 6], ex[8 * u + 7])};
      }
      l = x16_32_sum(l);
      if (lane < 4) { *(LAS float*)(L + L_ML + (lane * 4 + w) * 4) = l; if (w == 0) *(LAS float*)(L + L_ML + 64 + lane * 4) = M; }
      lds_arrive(lbase, C_PP);
    }
    STAMP(6);
    wg_barrier();
    STAMP(7);
    {
      const u32 oo = WS_O + (((xx * 8 + 2 * w + (lane >> 5)) * 4) * 128 + db * 32 + (lane & 31)) * 16;
      const u32 off[4] = {oo, oo + 2048, oo + 4096, oo + 6144};
      v4u go[4];
      if (w == 0) {
        const u32 mo = WS_ML + (xx * 128 + lane) * 16;
        const u32 moff[2] = {mo, mo + 1024};
        v4u gm[2];
        __builtin_amdgcn_s_sleep(MSLEEP0);
        SPINS(13, (gather<2, MSLEEP>(rs, moff, gm, tg)));
        lds_flag(lbase, F_ML);
#pragma unroll
        for (int s = 0; s < 4; ++s) go[s] = ld_g(rs, off[s]);
#pragma unroll
        for (int r = 0; r < 2; ++r) {
          const float ms = u2f(gm[r][0] & 0xfffff000u), ls = u2f(gm[r][1] & 0xfffff000u);
          float M = fmaxf(ms, dppm<0x124>(ms));
          M = fmaxf(M, dppm<0x128>(M));
          const float ew = __builtin_amdgcn_exp2f((ms - M) * 1.4426950408889634f);
          float Ls = ew * ls;
          Ls += dppm<0x124>(Ls);
          Ls += dppm<0x128>(Ls);
          *(LAS float*)(L + L_WT + (((4 * r + g) * 4 + (lane & 3)) * 4 + ((lane >> 2) & 3)) * 4) = ew * __builtin_amdgcn_rcpf(Ls);
        }
        lds_arrive(lbase, F_ML);
      } else {
        lds_wait<0>(L, F_ML, 1);
#pragma unroll
        for (int s = 0; s < 4; ++s) go[s] = ld_g(rs, off[s]);
      }
      STAMP(8);
      {
        u32 acc0 = chk(chk(0u, go[0], tg), go[1], tg), acc1 = chk(chk(0u, go[2], tg), go[3], tg);
        if (!__all(((acc0 | acc1) & 0xfffu) == 0u)) (void)gather<4, 0>(rs, off, go, tg);
      }
      lds_wait<0>(L, F_ML, 2);
      const f32x4 wt = *(LAS f32x4*)(L + L_WT + ((2 * w + (lane >> 5)) * 4 + (lane & 3)) * 16);
      const f32x4 O = (val_g(go[0]) * wt[0] + val_g(go[1]) * wt[1]) + (val_g(go[2]) * wt[2] + val_g(go[3]) * wt[3]);
      *(LAS v2u*)(L + L_XQ + (2 * w + (lane >> 5)) * 288 + ((lane & 3) * 32 + ((lane >> 2) & 7) * 4) * 2) = v2u{pk2(O[0], O[1]), pk2(O[2], O[3])};
      STAMP(9);
      wg_barrier();
    }
    if (w == 0) {
      const u32 fo = WS_F + ((((ns * 4 + fnt) * 2 + fG) * 32 + (lane >> 2)) * 16 + 4 * fcq + (lane & 3)) * 16;
      const u32 off[2] = {fo, fo + 16 * 256};
      v4u gg[2];
      __builtin_amdgcn_s_sleep(FSLEEP0);
      SPINS(14, (gather<2, FSLEEP>(rs, off, gg, tg)));
      const f32x4 v = val_g(gg[0]) + val_g(gg[1]);
      auto a02 = __builtin_amdgcn_permlane32_swap(f2u(v[0]), f2u(v[2]), false, false);
      auto a13 = __builtin_amdgcn_permlane32_swap(f2u(v[1]), f2u(v[3]), false, false);
      const u32 p0 = a02[0], p1 = a02[1], p2 = a13[0], p3 = a13[1];
      const float lo = u2f(p0) + u2f(p1), hi = u2f(p2) + u2f(p3);
      auto a = __builtin_amdgcn_permlane16_swap(f2u(lo), f2u(hi), false, false);
      const u32 q0 = a[0], q1 = a[1];
      float t = u2f(q0) + u2f(q1);
      t += dppm<0x124>(t);
      t += dppm<0x128>(t);
      const u32 ob = pk2(bflo(xres) + t, 0.f);
      if ((lane & 12) == 0) {
        *(GAS unsigned short*)(p.out + (fb * DM + fcol) * 2) = (unsigned short)ob;
      }
      if (wg == 0 && lane == 0) __hip_atomic_store((GAS u64*)(p.ws + WS_EPOCH), e, __ATOMIC_RELAXED, __HIP_MEMORY_SCOPE_AGENT);
      STAMP(15);
    }
    STAMPS_OUT();
    __builtin_amdgcn_s_waitcnt(0);
    __builtin_amdgcn_endpgm();
    __builtin_unreachable();
  }
}

}

extern "C" void kernel_launch(void* const* d_in, const int* in_sizes, int n_in, void* d_out, int out_size, void* d_ws,
                              size_t ws_size, hipStream_t stream) {
  (void)in_sizes; (void)n_in; (void)out_size; (void)ws_size;
  Params p;
  p.x = (const char*)d_in[0];
  p.kc = (const char*)d_in[1];
  p.vc = (const char*)d_in[2];
  p.wq = (const char*)d_in[3];
  p.wo = (const char*)d_in[4];
  p.w1 = (const char*)d_in[6];
  p.w2 = (const char*)d_in[7];
  p.out = (char*)d_out;
  p.ws = (char*)d_ws;
  hipLaunchKernelGGL(fused_decode_kernel, dim3(NWG), dim3(NTHR), 0, stream, p);
}
```

```cpp
#include <hip/hip_runtime.h>
#include <stdint.h>

#define GAS __attribute__((address_space(1)))
#define LAS __attribute__((address_space(3)))
typedef unsigned u32;
typedef unsigned long long u64;
typedef u32 v4u __attribute__((ext_vector_type(4)));
typedef u32 v2u __attribute__((ext_vector_type(2)));
typedef float f32x4 __attribute__((ext_vector_type(4)));
typedef float f32x2 __attribute__((ext_vector_type(2)));
typedef short s16x8 __attribute__((ext_vector_type(8)));
typedef short s16x4 __attribute__((ext_vector_type(4)));
typedef __bf16 bf16x2 __attribute__((ext_vector_type(2)));

#define KV_NT 1
#define W_NT 1
#define WIN 2
#define GSLEEP 6
#define LPRIO 3
#define FSLEEP0 20
#define FSLEEP 1
#define MSLEEP0 16
#define MSLEEP 1
#define WO_LATE 2
#define LFIRST 1
#define CPF 1
#define CPF_BACK 0
#define CPF_N 11
#define WQ_POL "nt"
#define OUT_SC1 1

namespace {

constexpr int DM = 512, HKV = 8, SK = 4096, DH = 128, WINDOW = 1024, FF = 1792;
constexpr int NWG = 256, NTHR = 768;

constexpr u32 WS_EPOCH = 0;
constexpr u32 WS_Q = 4096;
constexpr u32 WS_H = WS_Q + 8 * 8 * 4 * 128 * 16;
constexpr u32 WS_O = WS_H + 28 * 8 * 8 * 16 * 16;
constexpr u32 WS_ML = WS_O + 8 * 8 * 4 * 128 * 16;
constexpr u32 WS_F = WS_ML + 8 * 8 * 4 * 4 * 16;
constexpr u32 WS_END = WS_F + 8 * 4 * 2 * 32 * 16 * 16;
constexpr u32 WS_ST = WS_END;

constexpr u32 L_K = 0, L_V = 65536, L_W = 131072, L_W1 = L_W + 16384;
constexpr u32 L_XQ = L_W + 24576;
constexpr u32 L_XH = L_XQ + 8 * 288;
constexpr u32 L_QI = L_XH + 8 * 160;
constexpr u32 L_TR = L_QI + 4 * 288;
constexpr u32 L_ML = L_TR + 2048;
constexpr u32 L_MX = L_ML + 96;
constexpr u32 L_WT = L_MX + 64;
constexpr u32 L_FLAG = L_WT + 512;
constexpr u32 L_END = L_FLAG + 128;
static_assert(L_END <= 163840 && L_ML % 16 == 0 && L_MX % 16 == 0 && L_WT % 16 == 0 && L_FLAG % 16 == 0, "LDS");
constexpr u32 LDS_BYTES = 163840;
constexpr u32 C_A = L_FLAG, C_P = L_FLAG + 4, C_Q = L_FLAG + 8, C_M = L_FLAG + 12, C_W2 = L_FLAG + 16, C_W1 = L_FLAG + 20, C_H = L_FLAG + 24,
              C_PP = L_FLAG + 28, F_G = L_FLAG + 32, F_ML = L_FLAG + 36, C_HI = L_FLAG + 40;
constexpr u32 F_K = L_FLAG + 64;
constexpr u32 L_TAG = L_FLAG + 96;

constexpr u32 SPIN_CAP = 1u << 16;

struct Params {
  const char* x; const char* kc; const char* vc; const char* wq; const char* wo; const char* w1; const char* w2;
  char* out; char* ws;
};

__device__ __forceinline__ float u2f(u32 u) { return __builtin_bit_cast(float, u); }
__device__ __forceinline__ u32 f2u(float f) { return __builtin_bit_cast(u32, f); }
__device__ __forceinline__ float bflo(u32 u) { return u2f(u << 16); }
__device__ __forceinline__ float bfhi(u32 u) { return u2f(u & 0xffff0000u); }
__device__ __forceinline__ u32 pk2(float lo, float hi) {
  f32x2 v = {lo, hi};
  return __builtin_bit_cast(u32, __builtin_convertvector(v, bf16x2));
}
__device__ __forceinline__ float rbf(float f) { return bflo(pk2(f, f) & 0xffffu); }

template <int CTRL, int RM> __device__ __forceinline__ float dppz(float v) {
  return u2f((u32)__builtin_amdgcn_update_dpp(0, (int)f2u(v), CTRL, RM, 0xf, true));
}
template <int CTRL> __device__ __forceinline__ float dppm(float v) {
  return u2f((u32)__builtin_amdgcn_mov_dpp((int)f2u(v), CTRL, 0xf, 0xf, true));
}
__device__ __forceinline__ float x16_32_max(float x) {
  auto s = __builtin_amdgcn_permlane16_swap(f2u(x), f2u(x), false, false);
  const u32 s0 = s[0], s1 = s[1];
  x = fmaxf(u2f(s0), u2f(s1));
  auto t = __builtin_amdgcn_permlane32_swap(f2u(x), f2u(x), false, false);
  const u32 t0 = t[0], t1 = t[1];
  return fmaxf(u2f(t0), u2f(t1));
}
__device__ __forceinline__ float x16_32_sum(float x) {
  auto s = __builtin_amdgcn_permlane16_swap(f2u(x), f2u(x), false, false);
  const u32 s0 = s[0], s1 = s[1];
  x = u2f(s0) + u2f(s1);
  auto t = __builtin_amdgcn_permlane32_swap(f2u(x), f2u(x), false, false);
  const u32 t0 = t[0], t1 = t[1];
  return u2f(t0) + u2f(t1);
}

struct Tag { u32 t0, t1, t2, t3; };
__device__ __forceinline__ Tag make_tag(u64 e) {
  Tag t; t.t0 = (u32)e & 0xfffu; t.t1 = (u32)(e >> 12) & 0xfffu; t.t2 = t.t0; t.t3 = (u32)(e >> 24) & 0xfffu; return t;
}
__device__ __forceinline__ u32 pkw(float f, u32 t) { return ((f2u(f) + 0x800u) & 0xfffff000u) | t; }
__device__ __forceinline__ v4u pack_g(f32x4 v, const Tag& t) { return v4u{pkw(v[0], t.t0), pkw(v[1], t.t1), pkw(v[2], t.t2), pkw(v[3], t.t3)}; }
__device__ __forceinline__ f32x4 val_g(v4u g) {
  return f32x4{u2f(g[0] & 0xfffff000u), u2f(g[1] & 0xfffff000u), u2f(g[2] & 0xfffff000u), u2f(g[3] & 0xfffff000u)};
}
__device__ __forceinline__ u32 chk(u32 acc, v4u g, const Tag& t) {
  acc = __builtin_amdgcn_bitop3_b32(acc, g[0], t.t0, 0xF6);
  acc = __builtin_amdgcn_bitop3_b32(acc, g[1], t.t1, 0xF6);
  acc = __builtin_amdgcn_bitop3_b32(acc, g[2], t.t2, 0xF6);
  acc = __builtin_amdgcn_bitop3_b32(acc, g[3], t.t3, 0xF6);
  return acc;
}
constexpr int AUX_SC1 = 16;
__device__ __forceinline__ v4u ld_g(__amdgpu_buffer_rsrc_t rs, u32 off) { return __builtin_amdgcn_raw_buffer_load_b128(rs, off, 0, AUX_SC1); }
__device__ __forceinline__ void st_g(__amdgpu_buffer_rsrc_t rs, u32 off, v4u v) { __builtin_amdgcn_raw_buffer_store_b128(v, rs, off, 0, AUX_SC1); }
template <int N, int SLEEP> __device__ __forceinline__ u32 gather(__amdgpu_buffer_rsrc_t rs, const u32 (&off)[N], v4u (&g)[N], const Tag& tg) {
  u32 spin = 0;
  while (true) {
#pragma unroll
    for (int i = 0; i < N; ++i) g[i] = ld_g(rs, off[i]);
    u32 acc0 = 0, acc1 = 0;
#pragma unroll
    for (int i = 0; i < N; ++i) { if (i & 1) acc1 = chk(acc1, g[i], tg); else acc0 = chk(acc0, g[i], tg); }
    if (__all(((acc0 | acc1) & 0xfffu) == 0u) || ++spin > SPIN_CAP) break;
    if (SLEEP) __builtin_amdgcn_s_sleep(SLEEP);
    asm volatile("" ::: "memory");
  }
  return spin;
}

template <int NT> __device__ __forceinline__ void glds(const char* sbase, u32 voff, u32 dst) {
  if constexpr (NT)
    asm volatile("s_mov_b32 m0, %2\n\ts_nop 0\n\tglobal_load_lds_dwordx4 %0, %1 nt" ::"v"(voff), "s"(sbase), "s"(dst) : "memory");
  else
    asm volatile("s_mov_b32 m0, %2\n\ts_nop 0\n\tglobal_load_lds_dwordx4 %0, %1" ::"v"(voff), "s"(sbase), "s"(dst) : "memory");
}
template <int N> __device__ __forceinline__ void waitvm() { asm volatile("s_waitcnt vmcnt(%0)" ::"i"(N) : "memory"); }
__device__ __forceinline__ void wg_barrier() { asm volatile("s_waitcnt lgkmcnt(0)\n\ts_barrier" ::: "memory"); }

__device__ __forceinline__ f32x4 mfma(v4u a, v4u b, f32x4 c) {
  return __builtin_amdgcn_mfma_f32_16x16x32_bf16(__builtin_bit_cast(s16x8, a), __builtin_bit_cast(s16x8, b), c, 0, 0, 0);
}
__device__ __forceinline__ v4u tr_frag(LAS char* L, u32 a0, u32 a1) {
  const v2u x = __builtin_bit_cast(v2u, __builtin_amdgcn_ds_read_tr16_b64_v4i16((LAS s16x4*)(L + a0)));
  const v2u y = __builtin_bit_cast(v2u, __builtin_amdgcn_ds_read_tr16_b64_v4i16((LAS s16x4*)(L + a1)));
  return v4u{x[0], x[1], y[0], y[1]};
}
__device__ __forceinline__ v4u lds128(LAS char* L, u32 a) { return *(LAS v4u*)(L + a); }

__device__ __forceinline__ u32 lds_ld(LAS char* L, u32 a) { return *(volatile LAS u32*)(L + a); }
__device__ __forceinline__ void lds_st(LAS char* L, u32 a, u32 v) { *(volatile LAS u32*)(L + a) = v; }
__device__ __forceinline__ void lds_arrive(u32 lbase, u32 a) {
  const u32 addr = lbase + a, one = 1;
  asm volatile("s_waitcnt lgkmcnt(0)\n\ts_mov_b64 exec, 1\n\tds_add_u32 %0, %1\n\ts_mov_b64 exec, -1" ::"v"(addr), "v"(one) : "memory");
}
__device__ __forceinline__ void lds_flag(u32 lbase, u32 a) {
  const u32 addr = lbase + a, one = 1;
  asm volatile("s_waitcnt lgkmcnt(0)\n\ts_mov_b64 exec, 1\n\tds_write_b32 %0, %1\n\ts_mov_b64 exec, -1" ::"v"(addr), "v"(one) : "memory");
}
template <int SLEEP> __device__ __forceinline__ void lds_wait(LAS char* L, u32 a, u32 n) {
  while (lds_ld(L, a) < n) { if (SLEEP) __builtin_amdgcn_s_sleep(SLEEP); }
  asm volatile("" ::: "memory");
}

#define STAMP(k) do { } while (0)
#define STAMPS_OUT() do { } while (0)
#define SPINS(k, e) do { (void)(e); } while (0)

__global__ void __launch_bounds__(NTHR, 1) fused_decode_kernel(Params p) {
  __shared__ __attribute__((aligned(16))) char lds_raw[LDS_BYTES];
  LAS char* L = (LAS char*)lds_raw;
  const u32 lbase = (u32)(uintptr_t)L;
  const int tid = threadIdx.x, lane = tid & 63;
  const int wave = __builtin_amdgcn_readfirstlane(tid >> 6);
  const int wg = blockIdx.x, xx = wg & 7, j = wg >> 3;
  const int g = lane >> 4;
  asm volatile("" ::"s"(p.x), "s"(p.kc), "s"(p.vc), "s"(p.wq), "s"(p.wo), "s"(p.w1), "s"(p.w2), "s"(p.out), "s"(p.ws));
  STAMP(0);
  const int cs = j >> 2, ks = j & 3;
  const int db = j >> 3, ns = j & 7;
  const int hh = 4 * xx + db;
  const int bb = j >> 2, sp = j & 3;
  const bool ffn = xx < 7;
  const u32 trq = (u32)((lane >> 2) & 3), trp = (u32)(lane & 3);
  const u32 trf = ((trq >> 1) & 1) | (((u32)g & 1) << 1);
  const u32 tr_row = ((u32)g * 8 + trq) * 128 + trp * 8;

  const u32 wchunk = (u32)((lane & 7) ^ (((lane >> 4) & 1) << 1));
  const u32 wrow = (u32)(lane >> 3);
  const int fnt = hh & 3, fG = (hh >> 2) & 1, fcq = hh >> 3;
  const int fb = 4 * fG + 2 * (lane >> 5) + ((lane >> 4) & 1);
  const int fcol = ns * 64 + fnt * 16 + 4 * fcq + (lane & 3);
  v4u xv[2], code;
  v2u ev;
  u32 xres;
  {
    const int wl = (wave - 4) & 7;
    u64 pc;
    asm volatile("s_getpc_b64 %0" : "=s"(pc));
    const char* cb = (const char*)(pc & ~255ull);
    const u32 co = (u32)((j % CPF_N) * 1024 + lane * 16);
    const u32 xo = (u32)((wave & 3) * 2048 + lane * 16), zero = 0, ro = (u32)(fb * DM + fcol) * 2;
    const char* b0 = p.wq + ((size_t)(ks * 128 + 16 * wl) * 4096 + xx * 512 + cs * 64) * 2;
    const char* b1 = b0 + 8 * 8192;
    const u32 vo = wrow * 8192 + wchunk * 16, vo1 = vo ^ 64u;
    const u32 d0 = lbase + L_W + (2 * wl) * 1024, d1 = d0 + 1024;
    asm volatile(
        "s_cmp_lt_u32 %[wave], 4\n\ts_cselect_b64 exec, 0, -1\n\t"
        "s_mov_b32 m0, %[d0]\n\ts_nop 0\n\tglobal_load_lds_dwordx4 %[vo], %[b0] " WQ_POL "\n\t"
        "s_mov_b32 m0, %[d1]\n\ts_nop 0\n\tglobal_load_lds_dwordx4 %[vo1], %[b1] " WQ_POL "\n\t"
        "s_not_b64 exec, exec\n\t"
        "global_load_dwordx4 %[x0], %[xo], %[px]\n\t"
        "global_load_dwordx4 %[x1], %[xo], %[px] offset:1024\n\t"
        "global_load_dwordx2 %[ev], %[zero], %[ws] sc1\n\t"
        "global_load_ushort %[xr], %[ro], %[px]\n\t"
        "global_load_dwordx4 %[code], %[co], %[cb]\n\t"
        "s_mov_b64 exec, -1"
        : [x0] "=&v"(xv[0]), [x1] "=&v"(xv[1]), [ev] "=&v"(ev), [xr] "=&v"(xres), [code] "=&v"(code)
        : [wave] "s"(wave), [d0] "s"(d0), [d1] "s"(d1), [vo] "v"(vo), [vo1] "v"(vo1), [b0] "s"(b0), [b1] "s"(b1),
          [xo] "v"(xo), [px] "s"(p.x), [zero] "v"(zero), [ws] "s"(p.ws), [ro] "v"(ro), [co] "v"(co), [cb] "s"(cb)
        : "memory", "scc");
  }
  if (wave == 3 && lane < 32) lds_st(L, L_FLAG + lane * 4, 0u);
  wg_barrier();

  if (__builtin_expect(wave >= 4, LFIRST)) {
    __builtin_amdgcn_s_setprio(LPRIO);
    const int w = wave - 4;
    const size_t kvoff = ((((size_t)bb * HKV + xx) * SK + (SK - WINDOW) + sp * 256 + w * 32) * DH) * 2;
    const char* kb = p.kc + kvoff;
    const char* vb = p.vc + kvoff;
    const u32 r4 = (u32)(lane >> 4), c16 = (u32)(lane & 15);
    const u32 kvo = r4 * 256 + ((c16 ^ r4) * 16);
    const u32 vvo = r4 * 256 + ((c16 ^ (r4 << 1)) * 16);
    auto issueK = [&](int u) { const int pu = (u + j) & 7; glds<KV_NT>(kb + pu * 1024, kvo ^ (u32)((pu & 3) << 6), lbase + L_K + w * 8192 + pu * 1024); };
    auto issueV = [&](int u) { const int pu = (u + j) & 7; glds<KV_NT>(vb + pu * 1024, vvo ^ (u32)((pu & 1) << 7), lbase + L_V + w * 8192 + pu * 1024); };
    issueK(0); issueK(1); issueK(2);
    if (ffn) {
      const char* b1 = p.w1 + ((size_t)(ns * 64 + 8 * w) * FF + hh * 64) * 2;
      glds<W_NT>(b1, wrow * (FF * 2) + ((wchunk ^ ((u32)(w & 1) << 2)) * 16), lbase + L_W1 + w * 1024);
      waitvm<4>();
    } else {
      waitvm<3>();
    }
    lds_arrive(lbase, C_A);
    STAMP(1);
    issueK(3); waitvm<WIN>();
    issueK(4); waitvm<2>();
    lds_arrive(lbase, C_W1);
    issueK(5); waitvm<WIN>();
    issueK(6); waitvm<WIN>();
    issueK(7); waitvm<2>();
    if (w == 0) lds_flag(lbase, F_G);
    issueV(0); waitvm<WIN>();
    issueV(1); waitvm<2>();
    lds_flag(lbase, F_K + w * 4);
    STAMP(2);
    issueV(2); waitvm<WIN>();
    issueV(3); waitvm<WIN>();
    issueV(4); waitvm<WIN>();
    issueV(5); waitvm<WIN>();
    issueV(6); waitvm<WIN>();
    issueV(7);
    STAMP(3);
    lds_wait<1>(L, C_PP, 4);
    v4u P[8];
#pragma unroll
    for (int u = 0; u < 8; ++u) P[u] = lds128(L, L_K + u * 8192 + 4096 + g * 64 + (lane & 3) * 16);
    const f32x4 lsum = *(LAS f32x4*)(L + L_ML + (lane & 3) * 16);
    const float mx = *(LAS float*)(L + L_ML + 64 + (lane & 3) * 4);
    const v4u tv = lds128(L, L_TAG);
    const Tag tg{tv[0], tv[1], tv[2], tv[3]};
    const __amdgpu_buffer_rsrc_t rs = __builtin_amdgcn_make_buffer_rsrc((void*)p.ws, 0, (int)WS_END, 0x00020000);
    const u32 vrow = (u32)g * 4 + trq;
    u32 va = L_V + vrow * 256 + (((u32)w ^ (vrow & 7)) * 32) + trp * 8;
    asm volatile("" : "+v"(va));
    const int rec = (xx * 8 + bb) * 4 + sp;
    const bool mlane = w == 0 && g == 0 && (lane >> 2) == 1;
    const u32 ooff = mlane ? WS_ML + (rec * 4 + (lane & 3)) * 16 : WS_O + (rec * 128 + (4 * w + g) * 4 + (lane & 3)) * 16;
    STAMP(4);
    const char* wob = p.wo + ((size_t)((4 * xx + (w >> 1)) * 128 + 32 * db + 16 * (w & 1)) * DM + ns * 64) * 2;
    const char* w2b = p.w2 + ((size_t)((ffn ? hh : 0) * 64 + 8 * w) * DM + ns * 64) * 2;
    const char* wob1 = wob + 8 * 1024;
    u32 wvo = wrow * 1024 + wchunk * 16, wvo1 = wvo ^ 64u, wvo2 = wrow * 1024 + ((wchunk ^ ((u32)(w & 1) << 2)) * 16);
    asm volatile("" : "+v"(wvo), "+v"(wvo1), "+v"(wvo2) : "s"(wob), "s"(wob1), "s"(w2b));
    lds_wait<0>(L, C_P, 4);
    lds_wait<0>(L, C_H, 4);
    waitvm<0>();
    glds<W_NT>(wob, wvo, lbase + L_W + (2 * w) * 1024);
    glds<W_NT>(wob1, wvo1, lbase + L_W + (2 * w + 1) * 1024);
    if (ffn) glds<W_NT>(w2b, wvo2, lbase + L_W1 + w * 1024);
    STAMP(5);
    wg_barrier();
    STAMP(6);
    {
      v4u A[8];
#pragma unroll
      for (int u = 0; u < 8; ++u) A[u] = tr_frag(L, va + u * 8192, va + u * 8192 + 4096);
      __builtin_amdgcn_sched_barrier(0);
      f32x4 o = {0.f, 0.f, 0.f, 0.f};
#pragma unroll
      for (int u = 0; u < 8; ++u) o = mfma(A[u], P[u], o);
      if (mlane) o = f32x4{mx, (lsum[0] + lsum[1]) + (lsum[2] + lsum[3]), 0.f, 0.f};
      if ((lane & 15) < 4 || mlane) st_g(rs, ooff, pack_g(o, tg));
    }
    STAMP(7);
    if (w >= 4) {
      if (ffn) {
        const int gi = 32 * (w - 4) + (lane & 31);
        const u32 ho = WS_H + ((hh * 8 + 4 * (lane >> 5)) * 128 + gi) * 16;
        const u32 off[4] = {ho, ho + 2048, ho + 4096, ho + 6144};
        v4u ga[4];
        SPINS(12, (gather<4, 2>(rs, off, ga, tg)));
        const f32x4 a = (val_g(ga[0]) + val_g(ga[1])) + (val_g(ga[2]) + val_g(ga[3]));
        float r[4];
#pragma unroll
        for (int q = 0; q < 4; ++q) {
          auto sw = __builtin_amdgcn_permlane32_swap(f2u(a[q]), f2u(a[q]), false, false);
          const u32 s0 = sw[0], s1 = sw[1];
          const float hv = rbf(u2f(s0) + u2f(s1));
          r[q] = hv * __builtin_amdgcn_rcpf(1.f + __builtin_amdgcn_exp2f(-hv * 1.4426950408889634f));
        }
        if (lane < 32) *(LAS v2u*)(L + L_XH + (gi >> 4) * 160 + (gi & 15) * 8) = v2u{pk2(r[0], r[1]), pk2(r[2], r[3])};
        lds_arrive(lbase, C_HI);
      }
      waitvm<0>();
      lds_arrive(lbase, C_W2);
      STAMP(8);
    } else {
      waitvm<0>();
      lds_arrive(lbase, C_W2);
      lds_wait<1>(L, C_W2, WO_LATE == 1 ? 4 : 8);
      const int nt = w;
      const u32 a0 = tr_row + (((u32)nt ^ trf) * 32);
      v4u A[4];
#pragma unroll
      for (int t = 0; t < 4; ++t) A[t] = tr_frag(L, L_W + a0 + t * 4096, L_W + a0 + t * 4096 + 512);
      f32x4 acc = {0.f, 0.f, 0.f, 0.f};
      if (ffn) {
        v4u A2[2];
#pragma unroll
        for (int t = 0; t < 2; ++t) A2[t] = tr_frag(L, L_W1 + a0 + t * 4096, L_W1 + a0 + t * 4096 + 512);
        lds_wait<1>(L, C_HI, 4);
#pragma unroll
        for (int t = 0; t < 2; ++t) acc = mfma(lds128(L, L_XH + (lane & 7) * 160 + (4 * t + g) * 16), A2[t], acc);
      }
      u32 foff = WS_F + ((((ns * 4 + nt) * 2 + g) * 32 + hh) * 16 + (lane & 15)) * 16;
      u32 xqa = L_XQ + (lane & 7) * 288 + g * 16;
      asm volatile("" : "+v"(foff), "+v"(xqa));
      wg_barrier();
      STAMP(8);
      v4u B[4];
#pragma unroll
      for (int t = 0; t < 4; ++t) B[t] = lds128(L, xqa + t * 64);
      __builtin_amdgcn_sched_barrier(0);
#pragma unroll
      for (int t = 0; t < 4; ++t) acc = mfma(B[t], A[t], acc);
      if (lane < 32) st_g(rs, foff, pack_g(acc, tg));
      STAMP(9);
    }
    STAMPS_OUT();
    __builtin_amdgcn_s_waitcnt(0);
  } else {
    const int w = wave;
    const __amdgpu_buffer_rsrc_t rs = __builtin_amdgcn_make_buffer_rsrc((void*)p.ws, 0, (int)WS_END, 0x00020000);
    asm volatile("s_waitcnt vmcnt(3)" : "+v"(xv[0]), "+v"(xv[1])::"memory");
    STAMP(11);
    {
      float v[2][8], s1[2], s2[2], piv[2];
#pragma unroll
      for (int r = 0; r < 2; ++r) {
#pragma unroll
        for (int q = 0; q < 4; ++q) { v[r][2 * q] = bflo(xv[r][q]); v[r][2 * q + 1] = bfhi(xv[r][q]); }
        piv[r] = u2f(__builtin_amdgcn_readfirstlane(f2u(v[r][0])));
        float d[8];
#pragma unroll
        for (int q = 0; q < 8; ++q) d[q] = v[r][q] - piv[r];
        s1[r] = ((d[0] + d[1]) + (d[2] + d[3])) + ((d[4] + d[5]) + (d[6] + d[7]));
        s2[r] = fmaf(d[1], d[1], d[0] * d[0]) + fmaf(d[3], d[3], d[2] * d[2]) + (fmaf(d[5], d[5], d[4] * d[4]) + fmaf(d[7], d[7], d[6] * d[6]));
      }
#define RED4(C, RM) { const float a = dppz<C, RM>(s1[0]), b = dppz<C, RM>(s2[0]), c = dppz<C, RM>(s1[1]), d = dppz<C, RM>(s2[1]); s1[0] += a; s2[0] += b; s1[1] += c; s2[1] += d; }
      RED4(0x111, 0xf) RED4(0x112, 0xf) RED4(0x114, 0xf) RED4(0x118, 0xf) RED4(0x142, 0xa) RED4(0x143, 0xc)
#undef RED4
#pragma unroll
      for (int r = 0; r < 2; ++r) {
        const float t1 = u2f(__builtin_amdgcn_readlane(f2u(s1[r]), 63)), t2 = u2f(__builtin_amdgcn_readlane(f2u(s2[r]), 63));
        const float md = t1 * (1.f / DM);
        const float var = rbf(fmaxf(fmaf(-md, md, t2 * (1.f / DM)), 0.f));
        const float mean = rbf(piv[r] + md);
        const float den = rbf(__builtin_amdgcn_sqrtf(rbf(var + 1e-5f)));
        const float rd = __builtin_amdgcn_rcpf(den);
        v4u xn;
#pragma unroll
        for (int q = 0; q < 4; ++q) {
          const u32 d2 = pk2(v[r][2 * q] - mean, v[r][2 * q + 1] - mean);
          xn[q] = pk2(bflo(d2) * rd, bfhi(d2) * rd);
        }
        if ((lane >> 4) == ks) *(LAS v4u*)(L + L_XQ + (2 * w + r) * 288 + (lane & 15) * 16) = xn;
        if ((lane >> 3) == ns) *(LAS v4u*)(L + L_XH + (2 * w + r) * 160 + (lane & 7) * 16) = xn;
      }
    }
    asm volatile("s_waitcnt vmcnt(0)" : "+v"(ev), "+v"(xres), "+v"(code)::"memory");
    const u64 e = (((u64)(u32)__builtin_amdgcn_readfirstlane(ev[1]) << 32) | (u32)__builtin_amdgcn_readfirstlane(ev[0])) + 1;
    const Tag tg = make_tag(e);
    if (w == 0 && lane == 0) *(LAS v4u*)(L + L_TAG) = v4u{tg.t0, tg.t1, tg.t2, tg.t3};
    lds_arrive(lbase, C_A);
    STAMP(1);
    LAS char* T = L + L_TR + w * 512;
    const int tb = (lane >> 2) & 7, tG = lane & 3;
    const u32 a0 = tr_row + (((u32)w ^ trf) * 32);
    lds_wait<1>(L, C_A, 12);
    STAMP(2);
    {
      v4u A[4], B[4];
#pragma unroll
      for (int t = 0; t < 4; ++t) { A[t] = tr_frag(L, L_W + a0 + t * 4096, L_W + a0 + t * 4096 + 512); B[t] = lds128(L, L_XQ + (lane & 7) * 288 + (4 * t + g) * 16); }
      __builtin_amdgcn_sched_barrier(0);
      f32x4 acc = {0.f, 0.f, 0.f, 0.f};
#pragma unroll
      for (int t = 0; t < 4; ++t) acc = mfma(A[t], B[t], acc);
      if ((lane & 15) < 8) *(LAS f32x4*)(T + (lane & 7) * 64 + g * 16) = acc;
      const f32x4 tq = *(LAS f32x4*)(T + (lane & 31) * 16);
      if (lane < 32) st_g(rs, WS_Q + (((xx * 8 + tb) * 4 + ks) * 128 + cs * 16 + w * 4 + tG) * 16, pack_g(tq, tg));
    }
    lds_arrive(lbase, C_P);
    STAMP(3);
    if (ffn) {
      lds_wait<1>(L, C_W1, 8);
      v4u A[2], B[2];
#pragma unroll
      for (int t = 0; t < 2; ++t) { A[t] = tr_frag(L, L_W1 + a0 + t * 4096, L_W1 + a0 + t * 4096 + 512); B[t] = lds128(L, L_XH + (lane & 7) * 160 + (4 * t + g) * 16); }
      f32x4 acc = {0.f, 0.f, 0.f, 0.f};
#pragma unroll
      for (int t = 0; t < 2; ++t) acc = mfma(A[t], B[t], acc);
      if ((lane & 15) < 8) *(LAS f32x4*)(T + (lane & 7) * 64 + g * 16) = acc;
      const f32x4 th = *(LAS f32x4*)(T + (lane & 31) * 16);
      if (lane < 32) st_g(rs, WS_H + (((hh * 8 + ns) * 8 + tb) * 16 + w * 4 + tG) * 16, pack_g(th, tg));
    }
    lds_arrive(lbase, C_H);
    if (w < 2) {
      lds_wait<2>(L, F_G, 1);
      const u32 qo = WS_Q + ((xx * 8 + bb) * 4 * 128 + w * 64 + lane) * 16;
      const u32 off[4] = {qo, qo + 2048, qo + 4096, qo + 6144};
      v4u ga[4];
      SPINS(12, (gather<4, GSLEEP>(rs, off, ga, tg)));
      const f32x4 a = (val_g(ga[0]) + val_g(ga[1])) + (val_g(ga[2]) + val_g(ga[3]));
      *(LAS v2u*)(L + L_QI + (2 * w + (lane >> 5)) * 288 + (lane & 31) * 8) = v2u{pk2(a[0], a[1]), pk2(a[2], a[3])};
      lds_arrive(lbase, C_Q);
    }
    STAMP(4);
    lds_wait<1>(L, C_Q, 2);
    lds_wait<1>(L, F_K + (2 * w) * 4, 1);
    lds_wait<1>(L, F_K + (2 * w + 1) * 4, 1);
    STAMP(5);
    {
      const u32 c16 = (u32)(lane & 15);
      v4u B[4], A0[2][4], A1[2][4];
#pragma unroll
      for (int t = 0; t < 4; ++t) B[t] = lds128(L, L_QI + (lane & 3) * 288 + (4 * t + g) * 16);
#pragma unroll
      for (int u = 0; u < 2; ++u) {
        const u32 ka = L_K + (2 * w + u) * 8192 + c16 * 256 + (((u32)g ^ c16) * 16);
#pragma unroll
        for (int t = 0; t < 4; ++t) { A0[u][t] = lds128(L, ka ^ (u32)(t << 6)); A1[u][t] = lds128(L, (ka ^ (u32)(t << 6)) + 4096); }
      }
      __builtin_amdgcn_sched_barrier(0);
      f32x4 sc[4];
#pragma unroll
      for (int u = 0; u < 4; ++u) sc[u] = f32x4{0.f, 0.f, 0.f, 0.f};
#pragma unroll
      for (int t = 0; t < 4; ++t) {
        sc[0] = mfma(A0[0][t], B[t], sc[0]); sc[1] = mfma(A1[0][t], B[t], sc[1]);
        sc[2] = mfma(A0[1][t], B[t], sc[2]); sc[3] = mfma(A1[1][t], B[t], sc[3]);
      }
      float s[16];
#pragma unroll
      for (int u = 0; u < 4; ++u) {
        const u32 a = pk2(sc[u][0], sc[u][1]), b = pk2(sc[u][2], sc[u][3]);
        const u32 c = pk2(bflo(a) * 0.08838834764831845f, bfhi(a) * 0.08838834764831845f);
        const u32 d = pk2(bflo(b) * 0.08838834764831845f, bfhi(b) * 0.08838834764831845f);
        s[4 * u] = bflo(c); s[4 * u + 1] = bfhi(c); s[4 * u + 2] = bflo(d); s[4 * u + 3] = bfhi(d);
      }
      float m = fmaxf(fmaxf(fmaxf(fmaxf(s[0], s[1]), fmaxf(s[2], s[3])), fmaxf(fmaxf(s[4], s[5]), fmaxf(s[6], s[7]))),
                      fmaxf(fmaxf(fmaxf(s[8], s[9]), fmaxf(s[10], s[11])), fmaxf(fmaxf(s[12], s[13]), fmaxf(s[14], s[15]))));
      m = x16_32_max(m);
      if (lane < 4) *(LAS float*)(L + L_MX + (lane * 4 + w) * 4) = m;
      lds_arrive(lbase, C_M);
      lds_wait<0>(L, C_M, 4);
      const f32x4 m0 = *(LAS f32x4*)(L + L_MX + (lane & 3) * 16);
      const float M = fmaxf(fmaxf(m0[0], m0[1]), fmaxf(m0[2], m0[3]));
      const float Ml = M * 1.4426950408889634f;
      float ex[16];
#pragma unroll
      for (int r = 0; r < 16; ++r) ex[r] = __builtin_amdgcn_exp2f(fmaf(s[r], 1.4426950408889634f, -Ml));
      float l = (((ex[0] + ex[1]) + (ex[2] + ex[3])) + ((ex[4] + ex[5]) + (ex[6] + ex[7]))) + (((ex[8] + ex[9]) + (ex[10] + ex[11])) + ((ex[12] + ex[13]) + (ex[14] + ex[15])));
      if ((lane & 15) < 4) {
#pragma unroll
        for (int u = 0; u < 2; ++u)
          *(LAS v4u*)(L + L_K + (2 * w + u) * 8192 + 4096 + g * 64 + (lane & 3) * 16) =
              v4u{pk2(ex[8 * u], ex[8 * u + 1]), pk2(ex[8 * u + 2], ex[8 * u + 3]), pk2(ex[8 * u + 4], ex[8 * u + 5]), pk2(ex[8 * u + 6], ex[8 * u + 7])};
      }
      l = x16_32_sum(l);
      if (lane < 4) { *(LAS float*)(L + L_ML + (lane * 4 + w) * 4) = l; if (w == 0) *(LAS float*)(L + L_ML + 64 + lane * 4) = M; }
      lds_arrive(lbase, C_PP);
    }
    STAMP(6);
    wg_barrier();
    STAMP(7);
    {
      const u32 oo = WS_O + (((xx * 8 + 2 * w + (lane >> 5)) * 4) * 128 + db * 32 + (lane & 31)) * 16;
      const u32 off[4] = {oo, oo + 2048, oo + 4096, oo + 6144};
      v4u go[4];
      if (w == 0) {
        const u32 mo = WS_ML + (xx * 128 + lane) * 16;
        const u32 moff[2] = {mo, mo + 1024};
        v4u gm[2];
        __builtin_amdgcn_s_sleep(MSLEEP0);
        SPINS(13, (gather<2, MSLEEP>(rs, moff, gm, tg)));
        lds_flag(lbase, F_ML);
#pragma unroll
        for (int s = 0; s < 4; ++s) go[s] = ld_g(rs, off[s]);
#pragma unroll
        for (int r = 0; r < 2; ++r) {
          const float ms = u2f(gm[r][0] & 0xfffff000u), ls = u2f(gm[r][1] & 0xfffff000u);
          float M = fmaxf(ms, dppm<0x124>(ms));
          M = fmaxf(M, dppm<0x128>(M));
          const float ew = __builtin_amdgcn_exp2f((ms - M) * 1.4426950408889634f);
          float Ls = ew * ls;
          Ls += dppm<0x124>(Ls);
          Ls += dppm<0x128>(Ls);
          *(LAS float*)(L + L_WT + (((4 * r + g) * 4 + (lane & 3)) * 4 + ((lane >> 2) & 3)) * 4) = ew * __builtin_amdgcn_rcpf(Ls);
        }
        lds_arrive(lbase, F_ML);
      } else {
        lds_wait<0>(L, F_ML, 1);
#pragma unroll
        for (int s = 0; s < 4; ++s) go[s] = ld_g(rs, off[s]);
      }
      STAMP(8);
      {
        u32 acc0 = chk(chk(0u, go[0], tg), go[1], tg), acc1 = chk(chk(0u, go[2], tg), go[3], tg);
        if (!__all(((acc0 | acc1) & 0xfffu) == 0u)) (void)gather<4, 0>(rs, off, go, tg);
      }
      lds_wait<0>(L, F_ML, 2);
      const f32x4 wt = *(LAS f32x4*)(L + L_WT + ((2 * w + (lane >> 5)) * 4 + (lane & 3)) * 16);
      const f32x4 O = (val_g(go[0]) * wt[0] + val_g(go[1]) * wt[1]) + (val_g(go[2]) * wt[2] + val_g(go[3]) * wt[3]);
      *(LAS v2u*)(L + L_XQ + (2 * w + (lane >> 5)) * 288 + ((lane & 3) * 32 + ((lane >> 2) & 7) * 4) * 2) = v2u{pk2(O[0], O[1]), pk2(O[2], O[3])};
      STAMP(9);
      wg_barrier();
    }
    if (w == 0) {
      const u32 fo = WS_F + ((((ns * 4 + fnt) * 2 + fG) * 32 + (lane >> 2)) * 16 + 4 * fcq + (lane & 3)) * 16;
      const u32 off[2] = {fo, fo + 16 * 256};
      v4u gg[2];
      __builtin_amdgcn_s_sleep(FSLEEP0);
      SPINS(14, (gather<2, FSLEEP>(rs, off, gg, tg)));
      const f32x4 v = val_g(gg[0]) + val_g(gg[1]);
      auto a02 = __builtin_amdgcn_permlane32_swap(f2u(v[0]), f2u(v[2]), false, false);
      auto a13 = __builtin_amdgcn_permlane32_swap(f2u(v[1]), f2u(v[3]), false, false);
      const u32 p0 = a02[0], p1 = a02[1], p2 = a13[0], p3 = a13[1];
      const float lo = u2f(p0) + u2f(p1), hi = u2f(p2) + u2f(p3);
      auto a = __builtin_amdgcn_permlane16_swap(f2u(lo), f2u(hi), false, false);
      const u32 q0 = a[0], q1 = a[1];
      float t = u2f(q0) + u2f(q1);
      t += dppm<0x124>(t);
      t += dppm<0x128>(t);
      const u32 ob = pk2(bflo(xres) + t, 0.f);
      if ((lane & 12) == 0) {
        __hip_atomic_store((GAS unsigned short*)(p.out + (fb * DM + fcol) * 2), (unsigned short)ob, __ATOMIC_RELAXED, __HIP_MEMORY_SCOPE_AGENT);
      }
      if (wg == 0 && lane == 0) __hip_atomic_store((GAS u64*)(p.ws + WS_EPOCH), e, __ATOMIC_RELAXED, __HIP_MEMORY_SCOPE_AGENT);
      STAMP(15);
    }
    STAMPS_OUT();
    __builtin_amdgcn_s_waitcnt(0);
  }
}

}

extern "C" void kernel_launch(void* const* d_in, const int* in_sizes, int n_in, void* d_out, int out_size, void* d_ws,
                              size_t ws_size, hipStream_t stream) {
  (void)in_sizes; (void)n_in; (void)out_size; (void)ws_size;
  Params p;
  p.x = (const char*)d_in[0];
  p.kc = (const char*)d_in[1];
  p.vc = (const char*)d_in[2];
  p.wq = (const char*)d_in[3];
  p.wo = (const char*)d_in[4];
  p.w1 = (const char*)d_in[6];
  p.w2 = (const char*)d_in[7];
  p.out = (char*)d_out;
  p.ws = (char*)d_ws;
  hipLaunchKernelGGL(fused_decode_kernel, dim3(NWG), dim3(NTHR), 0, stream, p);
}
```

```cpp
#define PROBE 1
#define PROBEW 4
#define PROBEINV 0
#include <hip/hip_runtime.h>
#include <stdint.h>

#define GAS __attribute__((address_space(1)))
#define LAS __attribute__((address_space(3)))
typedef unsigned u32;
typedef unsigned long long u64;
typedef u32 v4u __attribute__((ext_vector_type(4)));
typedef u32 v2u __attribute__((ext_vector_type(2)));
typedef float f32x4 __attribute__((ext_vector_type(4)));
typedef float f32x2 __attribute__((ext_vector_type(2)));
typedef short s16x8 __attribute__((ext_vector_type(8)));
typedef short s16x4 __attribute__((ext_vector_type(4)));
typedef __bf16 bf16x2 __attribute__((ext_vector_type(2)));

#define KV_NT 1
#define W_NT 1
#define WIN 2
#define GSLEEP 6
#define LPRIO 3
#define FSLEEP0 20
#define FSLEEP 1
#define MSLEEP0 16
#define MSLEEP 1
#define WO_LATE 2
#define LFIRST 1
#define CPF 1
#define CPF_BACK 0
#define CPF_N 11
#define WQ_POL "nt"
#define OUT_SC1 0

namespace {

constexpr int DM = 512, HKV = 8, SK = 4096, DH = 128, WINDOW = 1024, FF = 1792;
constexpr int NWG = 256, NTHR = 768;

constexpr u32 WS_EPOCH = 0;
constexpr u32 WS_Q = 4096;
constexpr u32 WS_H = WS_Q + 8 * 8 * 4 * 128 * 16;
constexpr u32 WS_O = WS_H + 28 * 8 * 8 * 16 * 16;
constexpr u32 WS_ML = WS_O + 8 * 8 * 4 * 128 * 16;
constexpr u32 WS_F = WS_ML + 8 * 8 * 4 * 4 * 16;
constexpr u32 WS_END = WS_F + 8 * 4 * 2 * 32 * 16 * 16;
constexpr u32 WS_ST = WS_END;

constexpr u32 L_K = 0, L_V = 65536, L_W = 131072, L_W1 = L_W + 16384;
constexpr u32 L_XQ = L_W + 24576;
constexpr u32 L_XH = L_XQ + 8 * 288;
constexpr u32 L_QI = L_XH + 8 * 160;
constexpr u32 L_TR = L_QI + 4 * 288;
constexpr u32 L_ML = L_TR + 2048;
constexpr u32 L_MX = L_ML + 96;
constexpr u32 L_WT = L_MX + 64;
constexpr u32 L_FLAG = L_WT + 512;
constexpr u32 L_END = L_FLAG + 128;
static_assert(L_END <= 163840 && L_ML % 16 == 0 && L_MX % 16 == 0 && L_WT % 16 == 0 && L_FLAG % 16 == 0, "LDS");
constexpr u32 LDS_BYTES = 163840;
constexpr u32 C_A = L_FLAG, C_P = L_FLAG + 4, C_Q = L_FLAG + 8, C_M = L_FLAG + 12, C_W2 = L_FLAG + 16, C_W1 = L_FLAG + 20, C_H = L_FLAG + 24,
              C_PP = L_FLAG + 28, F_G = L_FLAG + 32, F_ML = L_FLAG + 36, C_HI = L_FLAG + 40;
constexpr u32 F_K = L_FLAG + 64;
constexpr u32 L_TAG = L_FLAG + 96;

constexpr u32 SPIN_CAP = 1u << 16;

struct Params {
  const char* x; const char* kc; const char* vc; const char* wq; const char* wo; const char* w1; const char* w2;
  char* out; char* ws;
};

__device__ __forceinline__ float u2f(u32 u) { return __builtin_bit_cast(float, u); }
__device__ __forceinline__ u32 f2u(float f) { return __builtin_bit_cast(u32, f); }
__device__ __forceinline__ float bflo(u32 u) { return u2f(u << 16); }
__device__ __forceinline__ float bfhi(u32 u) { return u2f(u & 0xffff0000u); }
__device__ __forceinline__ u32 pk2(float lo, float hi) {
  f32x2 v = {lo, hi};
  return __builtin_bit_cast(u32, __builtin_convertvector(v, bf16x2));
}
__device__ __forceinline__ float rbf(float f) { return bflo(pk2(f, f) & 0xffffu); }

template <int CTRL, int RM> __device__ __forceinline__ float dppz(float v) {
  return u2f((u32)__builtin_amdgcn_update_dpp(0, (int)f2u(v), CTRL, RM, 0xf, true));
}
template <int CTRL> __device__ __forceinline__ float dppm(float v) {
  return u2f((u32)__builtin_amdgcn_mov_dpp((int)f2u(v), CTRL, 0xf, 0xf, true));
}
__device__ __forceinline__ float x16_32_max(float x) {
  auto s = __builtin_amdgcn_permlane16_swap(f2u(x), f2u(x), false, false);
  const u32 s0 = s[0], s1 = s[1];
  x = fmaxf(u2f(s0), u2f(s1));
  auto t = __builtin_amdgcn_permlane32_swap(f2u(x), f2u(x), false, false);
  const u32 t0 = t[0], t1 = t[1];
  return fmaxf(u2f(t0), u2f(t1));
}
__device__ __forceinline__ float x16_32_sum(float x) {
  auto s = __builtin_amdgcn_permlane16_swap(f2u(x), f2u(x), false, false);
  const u32 s0 = s[0], s1 = s[1];
  x = u2f(s0) + u2f(s1);
  auto t = __builtin_amdgcn_permlane32_swap(f2u(x), f2u(x), false, false);
  const u32 t0 = t[0], t1 = t[1];
  return u2f(t0) + u2f(t1);
}

struct Tag { u32 t0, t1, t2, t3; };
__device__ __forceinline__ Tag make_tag(u64 e) {
  Tag t; t.t0 = (u32)e & 0xfffu; t.t1 = (u32)(e >> 12) & 0xfffu; t.t2 = t.t0; t.t3 = (u32)(e >> 24) & 0xfffu; return t;
}
__device__ __forceinline__ u32 pkw(float f, u32 t) { return ((f2u(f) + 0x800u) & 0xfffff000u) | t; }
__device__ __forceinline__ v4u pack_g(f32x4 v, const Tag& t) { return v4u{pkw(v[0], t.t0), pkw(v[1], t.t1), pkw(v[2], t.t2), pkw(v[3], t.t3)}; }
__device__ __forceinline__ f32x4 val_g(v4u g) {
  return f32x4{u2f(g[0] & 0xfffff000u), u2f(g[1] & 0xfffff000u), u2f(g[2] & 0xfffff000u), u2f(g[3] & 0xfffff000u)};
}
__device__ __forceinline__ u32 chk(u32 acc, v4u g, const Tag& t) {
  acc = __builtin_amdgcn_bitop3_b32(acc, g[0], t.t0, 0xF6);
  acc = __builtin_amdgcn_bitop3_b32(acc, g[1], t.t1, 0xF6);
  acc = __builtin_amdgcn_bitop3_b32(acc, g[2], t.t2, 0xF6);
  acc = __builtin_amdgcn_bitop3_b32(acc, g[3], t.t3, 0xF6);
  return acc;
}
constexpr int AUX_SC1 = 16;
__device__ __forceinline__ v4u ld_g(__amdgpu_buffer_rsrc_t rs, u32 off) { return __builtin_amdgcn_raw_buffer_load_b128(rs, off, 0, AUX_SC1); }
__device__ __forceinline__ void st_g(__amdgpu_buffer_rsrc_t rs, u32 off, v4u v) { __builtin_amdgcn_raw_buffer_store_b128(v, rs, off, 0, AUX_SC1); }
template <int N, int SLEEP> __device__ __forceinline__ u32 gather(__amdgpu_buffer_rsrc_t rs, const u32 (&off)[N], v4u (&g)[N], const Tag& tg) {
  u32 spin = 0;
  while (true) {
#pragma unroll
    for (int i = 0; i < N; ++i) g[i] = ld_g(rs, off[i]);
    u32 acc0 = 0, acc1 = 0;
#pragma unroll
    for (int i = 0; i < N; ++i) { if (i & 1) acc1 = chk(acc1, g[i], tg); else acc0 = chk(acc0, g[i], tg); }
    if (__all(((acc0 | acc1) & 0xfffu) == 0u) || ++spin > SPIN_CAP) break;
    if (SLEEP) __builtin_amdgcn_s_sleep(SLEEP);
    asm volatile("" ::: "memory");
  }
  return spin;
}

template <int NT> __device__ __forceinline__ void glds(const char* sbase, u32 voff, u32 dst) {
  if constexpr (NT)
    asm volatile("s_mov_b32 m0, %2\n\ts_nop 0\n\tglobal_load_lds_dwordx4 %0, %1 nt" ::"v"(voff), "s"(sbase), "s"(dst) : "memory");
  else
    asm volatile("s_mov_b32 m0, %2\n\ts_nop 0\n\tglobal_load_lds_dwordx4 %0, %1" ::"v"(voff), "s"(sbase), "s"(dst) : "memory");
}
template <int N> __device__ __forceinline__ void waitvm() { asm volatile("s_waitcnt vmcnt(%0)" ::"i"(N) : "memory"); }
__device__ __forceinline__ void wg_barrier() { asm volatile("s_waitcnt lgkmcnt(0)\n\ts_barrier" ::: "memory"); }

__device__ __forceinline__ f32x4 mfma(v4u a, v4u b, f32x4 c) {
  return __builtin_amdgcn_mfma_f32_16x16x32_bf16(__builtin_bit_cast(s16x8, a), __builtin_bit_cast(s16x8, b), c, 0, 0, 0);
}
__device__ __forceinline__ v4u tr_frag(LAS char* L, u32 a0, u32 a1) {
  const v2u x = __builtin_bit_cast(v2u, __builtin_amdgcn_ds_read_tr16_b64_v4i16((LAS s16x4*)(L + a0)));
  const v2u y = __builtin_bit_cast(v2u, __builtin_amdgcn_ds_read_tr16_b64_v4i16((LAS s16x4*)(L + a1)));
  return v4u{x[0], x[1], y[0], y[1]};
}
__device__ __forceinline__ v4u lds128(LAS char* L, u32 a) { return *(LAS v4u*)(L + a); }

__device__ __forceinline__ u32 lds_ld(LAS char* L, u32 a) { return *(volatile LAS u32*)(L + a); }
__device__ __forceinline__ void lds_st(LAS char* L, u32 a, u32 v) { *(volatile LAS u32*)(L + a) = v; }
__device__ __forceinline__ void lds_arrive(u32 lbase, u32 a) {
  const u32 addr = lbase + a, one = 1;
  asm volatile("s_waitcnt lgkmcnt(0)\n\ts_mov_b64 exec, 1\n\tds_add_u32 %0, %1\n\ts_mov_b64 exec, -1" ::"v"(addr), "v"(one) : "memory");
}
__device__ __forceinline__ void lds_flag(u32 lbase, u32 a) {
  const u32 addr = lbase + a, one = 1;
  asm volatile("s_waitcnt lgkmcnt(0)\n\ts_mov_b64 exec, 1\n\tds_write_b32 %0, %1\n\ts_mov_b64 exec, -1" ::"v"(addr), "v"(one) : "memory");
}
template <int SLEEP> __device__ __forceinline__ void lds_wait(LAS char* L, u32 a, u32 n) {
  while (lds_ld(L, a) < n) { if (SLEEP) __builtin_amdgcn_s_sleep(SLEEP); }
  asm volatile("" ::: "memory");
}

#define NSTAMP 16
#define STAMP(k) do { if ((k) == PROBE) asm volatile("s_memrealtime %0\n\ts_waitcnt lgkmcnt(0)" : "=s"(stamps[k])::"memory"); } while (0)
#define STAMPS_OUT() do { if (wave == PROBEW && stamps[PROBE] != 0) { u64 now_; asm volatile("s_memrealtime %0\n\ts_waitcnt lgkmcnt(0)" : "=s"(now_)::"memory"); \
    const long long dt_ = (long long)(stamps[PROBE] - t_entry); const u64 until_ = now_ + (u64)(PROBEINV ? 10 * (4000 - dt_) : 10 * dt_); \
    do { __builtin_amdgcn_s_sleep(4); asm volatile("s_memrealtime %0\n\ts_waitcnt lgkmcnt(0)" : "=s"(now_)::"memory"); } while (now_ < until_); } } while (0)
#define SPINS(k, e) do { (void)(e); } while (0)

__global__ void __launch_bounds__(NTHR, 1) fused_decode_kernel(Params p) {
  __shared__ __attribute__((aligned(16))) char lds_raw[LDS_BYTES];
  LAS char* L = (LAS char*)lds_raw;
  const u32 lbase = (u32)(uintptr_t)L;
  const int tid = threadIdx.x, lane = tid & 63;
  const int wave = __builtin_amdgcn_readfirstlane(tid >> 6);
  const int wg = blockIdx.x, xx = wg & 7, j = wg >> 3;
  const int g = lane >> 4;
  u64 t_entry;
  asm volatile("s_memrealtime %0" : "=s"(t_entry));
  u64 stamps[NSTAMP];
#pragma unroll
  for (int k = 0; k < NSTAMP; ++k) stamps[k] = 0;
  asm volatile("s_waitcnt lgkmcnt(0)" : "+s"(t_entry));
  asm volatile("" ::"s"(p.x), "s"(p.kc), "s"(p.vc), "s"(p.wq), "s"(p.wo), "s"(p.w1), "s"(p.w2), "s"(p.out), "s"(p.ws));
  STAMP(0);
  const int cs = j >> 2, ks = j & 3;
  const int db = j >> 3, ns = j & 7;
  const int hh = 4 * xx + db;
  const int bb = j >> 2, sp = j & 3;
  const bool ffn = xx < 7;
  const u32 trq = (u32)((lane >> 2) & 3), trp = (u32)(lane & 3);
  const u32 trf = ((trq >> 1) & 1) | (((u32)g & 1) << 1);
  const u32 tr_row = ((u32)g * 8 + trq) * 128 + trp * 8;

  const u32 wchunk = (u32)((lane & 7) ^ (((lane >> 4) & 1) << 1));
  const u32 wrow = (u32)(lane >> 3);
  const int fnt = hh & 3, fG = (hh >> 2) & 1, fcq = hh >> 3;
  const int fb = 4 * fG + 2 * (lane >> 5) + ((lane >> 4) & 1);
  const int fcol = ns * 64 + fnt * 16 + 4 * fcq + (lane & 3);
  v4u xv[2], code;
  v2u ev;
  u32 xres;
  {
    const int wl = (wave - 4) & 7;
    u64 pc;
    asm volatile("s_getpc_b64 %0" : "=s"(pc));
    const char* cb = (const char*)(pc & ~255ull);
    const u32 co = (u32)((j % CPF_N) * 1024 + lane * 16);
    const u32 xo = (u32)((wave & 3) * 2048 + lane * 16), zero = 0, ro = (u32)(fb * DM + fcol) * 2;
    const char* b0 = p.wq + ((size_t)(ks * 128 + 16 * wl) * 4096 + xx * 512 + cs * 64) * 2;
    const char* b1 = b0 + 8 * 8192;
    const u32 vo = wrow * 8192 + wchunk * 16, vo1 = vo ^ 64u;
    const u32 d0 = lbase + L_W + (2 * wl) * 1024, d1 = d0 + 1024;
    asm volatile(
        "s_cmp_lt_u32 %[wave], 4\n\ts_cselect_b64 exec, 0, -1\n\t"
        "s_mov_b32 m0, %[d0]\n\ts_nop 0\n\tglobal_load_lds_dwordx4 %[vo], %[b0] " WQ_POL "\n\t"
        "s_mov_b32 m0, %[d1]\n\ts_nop 0\n\tglobal_load_lds_dwordx4 %[vo1], %[b1] " WQ_POL "\n\t"
        "s_not_b64 exec, exec\n\t"
        "global_load_dwordx4 %[x0], %[xo], %[px]\n\t"
        "global_load_dwordx4 %[x1], %[xo], %[px] offset:1024\n\t"
        "global_load_dwordx2 %[ev], %[zero], %[ws] sc1\n\t"
        "global_load_ushort %[xr], %[ro], %[px]\n\t"
        "global_load_dwordx4 %[code], %[co], %[cb]\n\t"
        "s_mov_b64 exec, -1"
        : [x0] "=&v"(xv[0]), [x1] "=&v"(xv[1]), [ev] "=&v"(ev), [xr] "=&v"(xres), [code] "=&v"(code)
        : [wave] "s"(wave), [d0] "s"(d0), [d1] "s"(d1), [vo] "v"(vo), [vo1] "v"(vo1), [b0] "s"(b0), [b1] "s"(b1),
          [xo] "v"(xo), [px] "s"(p.x), [zero] "v"(zero), [ws] "s"(p.ws), [ro] "v"(ro), [co] "v"(co), [cb] "s"(cb)
        : "memory", "scc");
  }
  if (wave == 3 && lane < 32) lds_st(L, L_FLAG + lane * 4, 0u);
  wg_barrier();

  if (__builtin_expect(wave >= 4, LFIRST)) {
    __builtin_amdgcn_s_setprio(LPRIO);
    const int w = wave - 4;
    const size_t kvoff = ((((size_t)bb * HKV + xx) * SK + (SK - WINDOW) + sp * 256 + w * 32) * DH) * 2;
    const char* kb = p.kc + kvoff;
    const char* vb = p.vc + kvoff;
    const u32 r4 = (u32)(lane >> 4), c16 = (u32)(lane & 15);
    const u32 kvo = r4 * 256 + ((c16 ^ r4) * 16);
    const u32 vvo = r4 * 256 + ((c16 ^ (r4 << 1)) * 16);
    auto issueK = [&](int u) { const int pu = (u + j) & 7; glds<KV_NT>(kb + pu * 1024, kvo ^ (u32)((pu & 3) << 6), lbase + L_K + w * 8192 + pu * 1024); };
    auto issueV = [&](int u) { const int pu = (u + j) & 7; glds<KV_NT>(vb + pu * 1024, vvo ^ (u32)((pu & 1) << 7), lbase + L_V + w * 8192 + pu * 1024); };
    issueK(0); issueK(1); issueK(2);
    if (ffn) {
      const char* b1 = p.w1 + ((size_t)(ns * 64 + 8 * w) * FF + hh * 64) * 2;
      glds<W_NT>(b1, wrow * (FF * 2) + ((wchunk ^ ((u32)(w & 1) << 2)) * 16), lbase + L_W1 + w * 1024);
      waitvm<4>();
    } else {
      waitvm<3>();
    }
    lds_arrive(lbase, C_A);
    STAMP(1);
    issueK(3); waitvm<WIN>();
    issueK(4); waitvm<2>();
    lds_arrive(lbase, C_W1);
    issueK(5); waitvm<WIN>();
    issueK(6); waitvm<WIN>();
    issueK(7); waitvm<2>();
    if (w == 0) lds_flag(lbase, F_G);
    issueV(0); waitvm<WIN>();
    issueV(1); waitvm<2>();
    lds_flag(lbase, F_K + w * 4);
    STAMP(2);
    issueV(2); waitvm<WIN>();
    issueV(3); waitvm<WIN>();
    issueV(4); waitvm<WIN>();
    issueV(5); waitvm<WIN>();
    issueV(6); waitvm<WIN>();
    issueV(7);
    STAMP(3);
    lds_wait<1>(L, C_PP, 4);
    v4u P[8];
#pragma unroll
    for (int u = 0; u < 8; ++u) P[u] = lds128(L, L_K + u * 8192 + 4096 + g * 64 + (lane & 3) * 16);
    const f32x4 lsum = *(LAS f32x4*)(L + L_ML + (lane & 3) * 16);
    const float mx = *(LAS float*)(L + L_ML + 64 + (lane & 3) * 4);
    const v4u tv = lds128(L, L_TAG);
    const Tag tg{tv[0], tv[1], tv[2], tv[3]};
    const __amdgpu_buffer_rsrc_t rs = __builtin_amdgcn_make_buffer_rsrc((void*)p.ws, 0, (int)WS_END, 0x00020000);
    const u32 vrow = (u32)g * 4 + trq;
    u32 va = L_V + vrow * 256 + (((u32)w ^ (vrow & 7)) * 32) + trp * 8;
    asm volatile("" : "+v"(va));
    const int rec = (xx * 8 + bb) * 4 + sp;
    const bool mlane = w == 0 && g == 0 && (lane >> 2) == 1;
    const u32 ooff = mlane ? WS_ML + (rec * 4 + (lane & 3)) * 16 : WS_O + (rec * 128 + (4 * w + g) * 4 + (lane & 3)) * 16;
    STAMP(4);
    const char* wob = p.wo + ((size_t)((4 * xx + (w >> 1)) * 128 + 32 * db + 16 * (w & 1)) * DM + ns * 64) * 2;
    const char* w2b = p.w2 + ((size_t)((ffn ? hh : 0) * 64 + 8 * w) * DM + ns * 64) * 2;
    const char* wob1 = wob + 8 * 1024;
    u32 wvo = wrow * 1024 + wchunk * 16, wvo1 = wvo ^ 64u, wvo2 = wrow * 1024 + ((wchunk ^ ((u32)(w & 1) << 2)) * 16);
    asm volatile("" : "+v"(wvo), "+v"(wvo1), "+v"(wvo2) : "s"(wob), "s"(wob1), "s"(w2b));
    lds_wait<0>(L, C_P, 4);
    lds_wait<0>(L, C_H, 4);
    waitvm<0>();
    glds<W_NT>(wob, wvo, lbase + L_W + (2 * w) * 1024);
    glds<W_NT>(wob1, wvo1, lbase + L_W + (2 * w + 1) * 1024);
    if (ffn) glds<W_NT>(w2b, wvo2, lbase + L_W1 + w * 1024);
    STAMP(5);
    wg_barrier();
    STAMP(6);
    {
      v4u A[8];
#pragma unroll
      for (int u = 0; u < 8; ++u) A[u] = tr_frag(L, va + u * 8192, va + u * 8192 + 4096);
      __builtin_amdgcn_sched_barrier(0);
      f32x4 o = {0.f, 0.f, 0.f, 0.f};
#pragma unroll
      for (int u = 0; u < 8; ++u) o = mfma(A[u], P[u], o);
      if (mlane) o = f32x4{mx, (lsum[0] + lsum[1]) + (lsum[2] + lsum[3]), 0.f, 0.f};
      if ((lane & 15) < 4 || mlane) st_g(rs, ooff, pack_g(o, tg));
    }
    STAMP(7);
    if (w >= 4) {
      if (ffn) {
        const int gi = 32 * (w - 4) + (lane & 31);
        const u32 ho = WS_H + ((hh * 8 + 4 * (lane >> 5)) * 128 + gi) * 16;
        const u32 off[4] = {ho, ho + 2048, ho + 4096, ho + 6144};
        v4u ga[4];
        SPINS(12, (gather<4, 2>(rs, off, ga, tg)));
        const f32x4 a = (val_g(ga[0]) + val_g(ga[1])) + (val_g(ga[2]) + val_g(ga[3]));
        float r[4];
#pragma unroll
        for (int q = 0; q < 4; ++q) {
          auto sw = __builtin_amdgcn_permlane32_swap(f2u(a[q]), f2u(a[q]), false, false);
          const u32 s0 = sw[0], s1 = sw[1];
          const float hv = rbf(u2f(s0) + u2f(s1));
          r[q] = hv * __builtin_amdgcn_rcpf(1.f + __builtin_amdgcn_exp2f(-hv * 1.4426950408889634f));
        }
        if (lane < 32) *(LAS v2u*)(L + L_XH + (gi >> 4) * 160 + (gi & 15) * 8) = v2u{pk2(r[0], r[1]), pk2(r[2], r[3])};
        lds_arrive(lbase, C_HI);
      }
      waitvm<0>();
      lds_arrive(lbase, C_W2);
      STAMP(8);
    } else {
      waitvm<0>();
      lds_arrive(lbase, C_W2);
      lds_wait<1>(L, C_W2, WO_LATE == 1 ? 4 : 8);
      const int nt = w;
      const u32 a0 = tr_row + (((u32)nt ^ trf) * 32);
      v4u A[4];
#pragma unroll
      for (int t = 0; t < 4; ++t) A[t] = tr_frag(L, L_W + a0 + t * 4096, L_W + a0 + t * 4096 + 512);
      f32x4 acc = {0.f, 0.f, 0.f, 0.f};
      if (ffn) {
        v4u A2[2];
#pragma unroll
        for (int t = 0; t < 2; ++t) A2[t] = tr_frag(L, L_W1 + a0 + t * 4096, L_W1 + a0 + t * 4096 + 512);
        lds_wait<1>(L, C_HI, 4);
#pragma unroll
        for (int t = 0; t < 2; ++t) acc = mfma(lds128(L, L_XH + (lane & 7) * 160 + (4 * t + g) * 16), A2[t], acc);
      }
      u32 foff = WS_F + ((((ns * 4 + nt) * 2 + g) * 32 + hh) * 16 + (lane & 15)) * 16;
      u32 xqa = L_XQ + (lane & 7) * 288 + g * 16;
      asm volatile("" : "+v"(foff), "+v"(xqa));
      wg_barrier();
      STAMP(8);
      v4u B[4];
#pragma unroll
      for (int t = 0; t < 4; ++t) B[t] = lds128(L, xqa + t * 64);
      __builtin_amdgcn_sched_barrier(0);
#pragma unroll
      for (int t = 0; t < 4; ++t) acc = mfma(B[t], A[t], acc);
      if (lane < 32) st_g(rs, foff, pack_g(acc, tg));
      STAMP(9);
    }
    STAMPS_OUT();
    __builtin_amdgcn_s_waitcnt(0);
  } else {
    const int w = wave;
    const __amdgpu_buffer_rsrc_t rs = __builtin_amdgcn_make_buffer_rsrc((void*)p.ws, 0, (int)WS_END, 0x00020000);
    asm volatile("s_waitcnt vmcnt(3)" : "+v"(xv[0]), "+v"(xv[1])::"memory");
    STAMP(11);
    {
      float v[2][8], s1[2], s2[2], piv[2];
#pragma unroll
      for (int r = 0; r < 2; ++r) {
#pragma unroll
        for (int q = 0; q < 4; ++q) { v[r][2 * q] = bflo(xv[r][q]); v[r][2 * q + 1] = bfhi(xv[r][q]); }
        piv[r] = u2f(__builtin_amdgcn_readfirstlane(f2u(v[r][0])));
        float d[8];
#pragma unroll
        for (int q = 0; q < 8; ++q) d[q] = v[r][q] - piv[r];
        s1[r] = ((d[0] + d[1]) + (d[2] + d[3])) + ((d[4] + d[5]) + (d[6] + d[7]));
        s2[r] = fmaf(d[1], d[1], d[0] * d[0]) + fmaf(d[3], d[3], d[2] * d[2]) + (fmaf(d[5], d[5], d[4] * d[4]) + fmaf(d[7], d[7], d[6] * d[6]));
      }
#define RED4(C, RM) { const float a = dppz<C, RM>(s1[0]), b = dppz<C, RM>(s2[0]), c = dppz<C, RM>(s1[1]), d = dppz<C, RM>(s2[1]); s1[0] += a; s2[0] += b; s1[1] += c; s2[1] += d; }
      RED4(0x111, 0xf) RED4(0x112, 0xf) RED4(0x114, 0xf) RED4(0x118, 0xf) RED4(0x142, 0xa) RED4(0x143, 0xc)
#undef RED4
#pragma unroll
      for (int r = 0; r < 2; ++r) {
        const float t1 = u2f(__builtin_amdgcn_readlane(f2u(s1[r]), 63)), t2 = u2f(__builtin_amdgcn_readlane(f2u(s2[r]), 63));
        const float md = t1 * (1.f / DM);
        const float var = rbf(fmaxf(fmaf(-md, md, t2 * (1.f / DM)), 0.f));
        const float mean = rbf(piv[r] + md);
        const float den = rbf(__builtin_amdgcn_sqrtf(rbf(var + 1e-5f)));
        const float rd = __builtin_amdgcn_rcpf(den);
        v4u xn;
#pragma unroll
        for (int q = 0; q < 4; ++q) {
          const u32 d2 = pk2(v[r][2 * q] - mean, v[r][2 * q + 1] - mean);
          xn[q] = pk2(bflo(d2) * rd, bfhi(d2) * rd);
        }
        if ((lane >> 4) == ks) *(LAS v4u*)(L + L_XQ + (2 * w + r) * 288 + (lane & 15) * 16) = xn;
        if ((lane >> 3) == ns) *(LAS v4u*)(L + L_XH + (2 * w + r) * 160 + (lane & 7) * 16) = xn;
      }
    }
    asm volatile("s_waitcnt vmcnt(0)" : "+v"(ev), "+v"(xres), "+v"(code)::"memory");
    const u64 e = (((u64)(u32)__builtin_amdgcn_readfirstlane(ev[1]) << 32) | (u32)__builtin_amdgcn_readfirstlane(ev[0])) + 1;
    const Tag tg = make_tag(e);
    if (w == 0 && lane == 0) *(LAS v4u*)(L + L_TAG) = v4u{tg.t0, tg.t1, tg.t2, tg.t3};
    lds_arrive(lbase, C_A);
    STAMP(1);
    LAS char* T = L + L_TR + w * 512;
    const int tb = (lane >> 2) & 7, tG = lane & 3;
    const u32 a0 = tr_row + (((u32)w ^ trf) * 32);
    lds_wait<1>(L, C_A, 12);
    STAMP(2);
    {
      v4u A[4], B[4];
#pragma unroll
      for (int t = 0; t < 4; ++t) { A[t] = tr_frag(L, L_W + a0 + t * 4096, L_W + a0 + t * 4096 + 512); B[t] = lds128(L, L_XQ + (lane & 7) * 288 + (4 * t + g) * 16); }
      __builtin_amdgcn_sched_barrier(0);
      f32x4 acc = {0.f, 0.f, 0.f, 0.f};
#pragma unroll
      for (int t = 0; t < 4; ++t) acc = mfma(A[t], B[t], acc);
      if ((lane & 15) < 8) *(LAS f32x4*)(T + (lane & 7) * 64 + g * 16) = acc;
      const f32x4 tq = *(LAS f32x4*)(T + (lane & 31) * 16);
      if (lane < 32) st_g(rs, WS_Q + (((xx * 8 + tb) * 4 + ks) * 128 + cs * 16 + w * 4 + tG) * 16, pack_g(tq, tg));
    }
    lds_arrive(lbase, C_P);
    STAMP(3);
    if (ffn) {
      lds_wait<1>(L, C_W1, 8);
      v4u A[2], B[2];
#pragma unroll
      for (int t = 0; t < 2; ++t) { A[t] = tr_frag(L, L_W1 + a0 + t * 4096, L_W1 + a0 + t * 4096 + 512); B[t] = lds128(L, L_XH + (lane & 7) * 160 + (4 * t + g) * 16); }
      f32x4 acc = {0.f, 0.f, 0.f, 0.f};
#pragma unroll
      for (int t = 0; t < 2; ++t) acc = mfma(A[t], B[t], acc);
      if ((lane & 15) < 8) *(LAS f32x4*)(T + (lane & 7) * 64 + g * 16) = acc;
      const f32x4 th = *(LAS f32x4*)(T + (lane & 31) * 16);
      if (lane < 32) st_g(rs, WS_H + (((hh * 8 + ns) * 8 + tb) * 16 + w * 4 + tG) * 16, pack_g(th, tg));
    }
    lds_arrive(lbase, C_H);
    if (w < 2) {
      lds_wait<2>(L, F_G, 1);
      const u32 qo = WS_Q + ((xx * 8 + bb) * 4 * 128 + w * 64 + lane) * 16;
      const u32 off[4] = {qo, qo + 2048, qo + 4096, qo + 6144};
      v4u ga[4];
      SPINS(12, (gather<4, GSLEEP>(rs, off, ga, tg)));
      const f32x4 a = (val_g(ga[0]) + val_g(ga[1])) + (val_g(ga[2]) + val_g(ga[3]));
      *(LAS v2u*)(L + L_QI + (2 * w + (lane >> 5)) * 288 + (lane & 31) * 8) = v2u{pk2(a[0], a[1]), pk2(a[2], a[3])};
      lds_arrive(lbase, C_Q);
    }
    STAMP(4);
    lds_wait<1>(L, C_Q, 2);
    lds_wait<1>(L, F_K + (2 * w) * 4, 1);
    lds_wait<1>(L, F_K + (2 * w + 1) * 4, 1);
    STAMP(5);
    {
      const u32 c16 = (u32)(lane & 15);
      v4u B[4], A0[2][4], A1[2][4];
#pragma unroll
      for (int t = 0; t < 4; ++t) B[t] = lds128(L, L_QI + (lane & 3) * 288 + (4 * t + g) * 16);
#pragma unroll
      for (int u = 0; u < 2; ++u) {
        const u32 ka = L_K + (2 * w + u) * 8192 + c16 * 256 + (((u32)g ^ c16) * 16);
#pragma unroll
        for (int t = 0; t < 4; ++t) { A0[u][t] = lds128(L, ka ^ (u32)(t << 6)); A1[u][t] = lds128(L, (ka ^ (u32)(t << 6)) + 4096); }
      }
      __builtin_amdgcn_sched_barrier(0);
      f32x4 sc[4];
#pragma unroll
      for (int u = 0; u < 4; ++u) sc[u] = f32x4{0.f, 0.f, 0.f, 0.f};
#pragma unroll
      for (int t = 0; t < 4; ++t) {
        sc[0] = mfma(A0[0][t], B[t], sc[0]); sc[1] = mfma(A1[0][t], B[t], sc[1]);
        sc[2] = mfma(A0[1][t], B[t], sc[2]); sc[3] = mfma(A1[1][t], B[t], sc[3]);
      }
      float s[16];
#pragma unroll
      for (int u = 0; u < 4; ++u) {
        const u32 a = pk2(sc[u][0], sc[u][1]), b = pk2(sc[u][2], sc[u][3]);
        const u32 c = pk2(bflo(a) * 0.08838834764831845f, bfhi(a) * 0.08838834764831845f);
        const u32 d = pk2(bflo(b) * 0.08838834764831845f, bfhi(b) * 0.08838834764831845f);
        s[4 * u] = bflo(c); s[4 * u + 1] = bfhi(c); s[4 * u + 2] = bflo(d); s[4 * u + 3] = bfhi(d);
      }
      float m = fmaxf(fmaxf(fmaxf(fmaxf(s[0], s[1]), fmaxf(s[2], s[3])), fmaxf(fmaxf(s[4], s[5]), fmaxf(s[6], s[7]))),
                      fmaxf(fmaxf(fmaxf(s[8], s[9]), fmaxf(s[10], s[11])), fmaxf(fmaxf(s[12], s[13]), fmaxf(s[14], s[15]))));
      m = x16_32_max(m);
      if (lane < 4) *(LAS float*)(L + L_MX + (lane * 4 + w) * 4) = m;
      lds_arrive(lbase, C_M);
      lds_wait<0>(L, C_M, 4);
      const f32x4 m0 = *(LAS f32x4*)(L + L_MX + (lane & 3) * 16);
      const float M = fmaxf(fmaxf(m0[0], m0[1]), fmaxf(m0[2], m0[3]));
      const float Ml = M * 1.4426950408889634f;
      float ex[16];
#pragma unroll
      for (int r = 0; r < 16; ++r) ex[r] = __builtin_amdgcn_exp2f(fmaf(s[r], 1.4426950408889634f, -Ml));
      float l = (((ex[0] + ex[1]) + (ex[2] + ex[3])) + ((ex[4] + ex[5]) + (ex[6] + ex[7]))) + (((ex[8] + ex[9]) + (ex[10] + ex[11])) + ((ex[12] + ex[13]) + (ex[14] + ex[15])));
      if ((lane & 15) < 4) {
#pragma unroll
        for (int u = 0; u < 2; ++u)
          *(LAS v4u*)(L + L_K + (2 * w + u) * 8192 + 4096 + g * 64 + (lane & 3) * 16) =
              v4u{pk2(ex[8 * u], ex[8 * u + 1]), pk2(ex[8 * u + 2], ex[8 * u + 3]), pk2(ex[8 * u + 4], ex[8 * u + 5]), pk2(ex[8 * u + 6], ex[8 * u + 7])};
      }
      l = x16_32_sum(l);
      if (lane < 4) { *(LAS float*)(L + L_ML + (lane * 4 + w) * 4) = l; if (w == 0) *(LAS float*)(L + L_ML + 64 + lane * 4) = M; }
      lds_arrive(lbase, C_PP);
    }
    STAMP(6);
    wg_barrier();
    STAMP(7);
    {
      const u32 oo = WS_O + (((xx * 8 + 2 * w + (lane >> 5)) * 4) * 128 + db * 32 + (lane & 31)) * 16;
      const u32 off[4] = {oo, oo + 2048, oo + 4096, oo + 6144};
      v4u go[4];
      if (w == 0) {
        const u32 mo = WS_ML + (xx * 128 + lane) * 16;
        const u32 moff[2] = {mo, mo + 1024};
        v4u gm[2];
        __builtin_amdgcn_s_sleep(MSLEEP0);
        SPINS(13, (gather<2, MSLEEP>(rs, moff, gm, tg)));
        lds_flag(lbase, F_ML);
#pragma unroll
        for (int s = 0; s < 4; ++s) go[s] = ld_g(rs, off[s]);
#pragma unroll
        for (int r = 0; r < 2; ++r) {
          const float ms = u2f(gm[r][0] & 0xfffff000u), ls = u2f(gm[r][1] & 0xfffff000u);
          float M = fmaxf(ms, dppm<0x124>(ms));
          M = fmaxf(M, dppm<0x128>(M));
          const float ew = __builtin_amdgcn_exp2f((ms - M) * 1.4426950408889634f);
          float Ls = ew * ls;
          Ls += dppm<0x124>(Ls);
          Ls += dppm<0x128>(Ls);
          *(LAS float*)(L + L_WT + (((4 * r + g) * 4 + (lane & 3)) * 4 + ((lane >> 2) & 3)) * 4) = ew * __builtin_amdgcn_rcpf(Ls);
        }
        lds_arrive(lbase, F_ML);
      } else {
        lds_wait<0>(L, F_ML, 1);
#pragma unroll
        for (int s = 0; s < 4; ++s) go[s] = ld_g(rs, off[s]);
      }
      STAMP(8);
      {
        u32 acc0 = chk(chk(0u, go[0], tg), go[1], tg), acc1 = chk(chk(0u, go[2], tg), go[3], tg);
        if (!__all(((acc0 | acc1) & 0xfffu) == 0u)) (void)gather<4, 0>(rs, off, go, tg);
      }
      lds_wait<0>(L, F_ML, 2);
      const f32x4 wt = *(LAS f32x4*)(L + L_WT + ((2 * w + (lane >> 5)) * 4 + (lane & 3)) * 16);
      const f32x4 O = (val_g(go[0]) * wt[0] + val_g(go[1]) * wt[1]) + (val_g(go[2]) * wt[2] + val_g(go[3]) * wt[3]);
      *(LAS v2u*)(L + L_XQ + (2 * w + (lane >> 5)) * 288 + ((lane & 3) * 32 + ((lane >> 2) & 7) * 4) * 2) = v2u{pk2(O[0], O[1]), pk2(O[2], O[3])};
      STAMP(9);
      wg_barrier();
    }
    if (w == 0) {
      const u32 fo = WS_F + ((((ns * 4 + fnt) * 2 + fG) * 32 + (lane >> 2)) * 16 + 4 * fcq + (lane & 3)) * 16;
      const u32 off[2] = {fo, fo + 16 * 256};
      v4u gg[2];
      __builtin_amdgcn_s_sleep(FSLEEP0);
      SPINS(14, (gather<2, FSLEEP>(rs, off, gg, tg)));
      const f32x4 v = val_g(gg[0]) + val_g(gg[1]);
      auto a02 = __builtin_amdgcn_permlane32_swap(f2u(v[0]), f2u(v[2]), false, false);
      auto a13 = __builtin_amdgcn_permlane32_swap(f2u(v[1]), f2u(v[3]), false, false);
      const u32 p0 = a02[0], p1 = a02[1], p2 = a13[0], p3 = a13[1];
      const float lo = u2f(p0) + u2f(p1), hi = u2f(p2) + u2f(p3);
      auto a = __builtin_amdgcn_permlane16_swap(f2u(lo), f2u(hi), false, false);
      const u32 q0 = a[0], q1 = a[1];
      float t = u2f(q0) + u2f(q1);
      t += dppm<0x124>(t);
      t += dppm<0x128>(t);
      const u32 ob = pk2(bflo(xres) + t, 0.f);
      if ((lane & 12) == 0) {
        *(GAS unsigned short*)(p.out + (fb * DM + fcol) * 2) = (unsigned short)ob;
      }
      if (wg == 0 && lane == 0) __hip_atomic_store((GAS u64*)(p.ws + WS_EPOCH), e, __ATOMIC_RELAXED, __HIP_MEMORY_SCOPE_AGENT);
      STAMP(15);
    }
    STAMPS_OUT();
    __builtin_amdgcn_s_waitcnt(0);
  }
}

}

extern "C" void kernel_launch(void* const* d_in, const int* in_sizes, int n_in, void* d_out, int out_size, void* d_ws,
                              size_t ws_size, hipStream_t stream) {
  (void)in_sizes; (void)n_in; (void)out_size; (void)ws_size;
  Params p;
  p.x = (const char*)d_in[0];
  p.kc = (const char*)d_in[1];
  p.vc = (const char*)d_in[2];
  p.wq = (const char*)d_in[3];
  p.wo = (const char*)d_in[4];
  p.w1 = (const char*)d_in[6];
  p.w2 = (const char*)d_in[7];
  p.out = (char*)d_out;
  p.ws = (char*)d_ws;
  hipLaunchKernelGGL(fused_decode_kernel, dim3(NWG), dim3(NTHR), 0, stream, p);
}
```

```cpp
#include <hip/hip_runtime.h>
#include <stdint.h>

#define GAS __attribute__((address_space(1)))
#define LAS __attribute__((address_space(3)))
typedef unsigned u32;
typedef unsigned long long u64;
typedef u32 v4u __attribute__((ext_vector_type(4)));
typedef u32 v2u __attribute__((ext_vector_type(2)));
typedef float f32x4 __attribute__((ext_vector_type(4)));
typedef float f32x2 __attribute__((ext_vector_type(2)));
typedef short s16x8 __attribute__((ext_vector_type(8)));
typedef short s16x4 __attribute__((ext_vector_type(4)));
typedef __bf16 bf16x2 __attribute__((ext_vector_type(2)));

#define KV_NT 1
#define W_NT 1
#define WIN 2
#define GSLEEP 6
#define LPRIO 3
#define FSLEEP0 20
#define FSLEEP 1
#define MSLEEP0 16
#define MSLEEP 1
#define WO_LATE 2
#define LFIRST 1
#define CPF 1
#define CPF_BACK 0
#define CPF_N 12
#define CPF_BYTES 11520
#define WQ_POL "nt"
#define OUT_SC1 0

namespace {

constexpr int DM = 512, HKV = 8, SK = 4096, DH = 128, WINDOW = 1024, FF = 1792;
constexpr int NWG = 256, NTHR = 768;

constexpr u32 WS_EPOCH = 0;
constexpr u32 WS_Q = 4096;
constexpr u32 WS_H = WS_Q + 8 * 8 * 4 * 128 * 16;
constexpr u32 WS_O = WS_H + 28 * 8 * 8 * 16 * 16;
constexpr u32 WS_ML = WS_O + 8 * 8 * 4 * 128 * 16;
constexpr u32 WS_F = WS_ML + 8 * 8 * 4 * 4 * 16;
constexpr u32 WS_END = WS_F + 8 * 4 * 2 * 32 * 16 * 16;
constexpr u32 WS_ST = WS_END;

constexpr u32 L_K = 0, L_V = 65536, L_W = 131072, L_W1 = L_W + 16384;
constexpr u32 L_XQ = L_W + 24576;
constexpr u32 L_XH = L_XQ + 8 * 288;
constexpr u32 L_QI = L_XH + 8 * 160;
constexpr u32 L_TR = L_QI + 4 * 288;
constexpr u32 L_ML = L_TR + 2048;
constexpr u32 L_MX = L_ML + 96;
constexpr u32 L_WT = L_MX + 64;
constexpr u32 L_FLAG = L_WT + 512;
constexpr u32 L_END = L_FLAG + 128;
static_assert(L_END <= 163840 && L_ML % 16 == 0 && L_MX % 16 == 0 && L_WT % 16 == 0 && L_FLAG % 16 == 0, "LDS");
constexpr u32 LDS_BYTES = 163840;
constexpr u32 C_A = L_FLAG, C_P = L_FLAG + 4, C_Q = L_FLAG + 8, C_M = L_FLAG + 12, C_W2 = L_FLAG + 16, C_W1 = L_FLAG + 20, C_H = L_FLAG + 24,
              C_PP = L_FLAG + 28, F_G = L_FLAG + 32, F_ML = L_FLAG + 36, C_HI = L_FLAG + 40;
constexpr u32 F_K = L_FLAG + 64;
constexpr u32 L_TAG = L_FLAG + 96;

constexpr u32 SPIN_CAP = 1u << 16;

struct Params {
  const char* x; const char* kc; const char* vc; const char* wq; const char* wo; const char* w1; const char* w2;
  char* out; char* ws;
};

__device__ __forceinline__ float u2f(u32 u) { return __builtin_bit_cast(float, u); }
__device__ __forceinline__ u32 f2u(float f) { return __builtin_bit_cast(u32, f); }
__device__ __forceinline__ float bflo(u32 u) { return u2f(u << 16); }
__device__ __forceinline__ float bfhi(u32 u) { return u2f(u & 0xffff0000u); }
__device__ __forceinline__ u32 pk2(float lo, float hi) {
  f32x2 v = {lo, hi};
  return __builtin_bit_cast(u32, __builtin_convertvector(v, bf16x2));
}
__device__ __forceinline__ float rbf(float f) { return bflo(pk2(f, f) & 0xffffu); }

template <int CTRL, int RM> __device__ __forceinline__ float dppz(float v) {
  return u2f((u32)__builtin_amdgcn_update_dpp(0, (int)f2u(v), CTRL, RM, 0xf, true));
}
template <int CTRL> __device__ __forceinline__ float dppm(float v) {
  return u2f((u32)__builtin_amdgcn_mov_dpp((int)f2u(v), CTRL, 0xf, 0xf, true));
}
__device__ __forceinline__ float x16_32_max(float x) {
  auto s = __builtin_amdgcn_permlane16_swap(f2u(x), f2u(x), false, false);
  const u32 s0 = s[0], s1 = s[1];
  x = fmaxf(u2f(s0), u2f(s1));
  auto t = __builtin_amdgcn_permlane32_swap(f2u(x), f2u(x), false, false);
  const u32 t0 = t[0], t1 = t[1];
  return fmaxf(u2f(t0), u2f(t1));
}
__device__ __forceinline__ float x16_32_sum(float x) {
  auto s = __builtin_amdgcn_permlane16_swap(f2u(x), f2u(x), false, false);
  const u32 s0 = s[0], s1 = s[1];
  x = u2f(s0) + u2f(s1);
  auto t = __builtin_amdgcn_permlane32_swap(f2u(x), f2u(x), false, false);
  const u32 t0 = t[0], t1 = t[1];
  return u2f(t0) + u2f(t1);
}

struct Tag { u32 t0, t1, t2, t3; };
__device__ __forceinline__ Tag make_tag(u64 e) {
  Tag t; t.t0 = (u32)e & 0xfffu; t.t1 = (u32)(e >> 12) & 0xfffu; t.t2 = t.t0; t.t3 = (u32)(e >> 24) & 0xfffu; return t;
}
__device__ __forceinline__ u32 pkw(float f, u32 t) { return ((f2u(f) + 0x800u) & 0xfffff000u) | t; }
__device__ __forceinline__ v4u pack_g(f32x4 v, const Tag& t) { return v4u{pkw(v[0], t.t0), pkw(v[1], t.t1), pkw(v[2], t.t2), pkw(v[3], t.t3)}; }
__device__ __forceinline__ f32x4 val_g(v4u g) {
  return f32x4{u2f(g[0] & 0xfffff000u), u2f(g[1] & 0xfffff000u), u2f(g[2] & 0xfffff000u), u2f(g[3] & 0xfffff000u)};
}
__device__ __forceinline__ u32 chk(u32 acc, v4u g, const Tag& t) {
  acc = __builtin_amdgcn_bitop3_b32(acc, g[0], t.t0, 0xF6);
  acc = __builtin_amdgcn_bitop3_b32(acc, g[1], t.t1, 0xF6);
  acc = __builtin_amdgcn_bitop3_b32(acc, g[2], t.t2, 0xF6);
  acc = __builtin_amdgcn_bitop3_b32(acc, g[3], t.t3, 0xF6);
  return acc;
}
constexpr int AUX_SC1 = 16;
__device__ __forceinline__ v4u ld_g(__amdgpu_buffer_rsrc_t rs, u32 off) { return __builtin_amdgcn_raw_buffer_load_b128(rs, off, 0, AUX_SC1); }
__device__ __forceinline__ void st_g(__amdgpu_buffer_rsrc_t rs, u32 off, v4u v) { __builtin_amdgcn_raw_buffer_store_b128(v, rs, off, 0, AUX_SC1); }
template <int N, int SLEEP> __device__ __forceinline__ u32 gather(__amdgpu_buffer_rsrc_t rs, const u32 (&off)[N], v4u (&g)[N], const Tag& tg) {
  u32 spin = 0;
  while (true) {
#pragma unroll
    for (int i = 0; i < N; ++i) g[i] = ld_g(rs, off[i]);
    u32 acc0 = 0, acc1 = 0;
#pragma unroll
    for (int i = 0; i < N; ++i) { if (i & 1) acc1 = chk(acc1, g[i], tg); else acc0 = chk(acc0, g[i], tg); }
    if (__all(((acc0 | acc1) & 0xfffu) == 0u) || ++spin > SPIN_CAP) break;
    if (SLEEP) __builtin_amdgcn_s_sleep(SLEEP);
    asm volatile("" ::: "memory");
  }
  return spin;
}

template <int NT> __device__ __forceinline__ void glds(const char* sbase, u32 voff, u32 dst) {
  if constexpr (NT)
    asm volatile("s_mov_b32 m0, %2\n\ts_nop 0\n\tglobal_load_lds_dwordx4 %0, %1 nt" ::"v"(voff), "s"(sbase), "s"(dst) : "memory");
  else
    asm volatile("s_mov_b32 m0, %2\n\ts_nop 0\n\tglobal_load_lds_dwordx4 %0, %1" ::"v"(voff), "s"(sbase), "s"(dst) : "memory");
}
template <int N> __device__ __forceinline__ void waitvm() { asm volatile("s_waitcnt vmcnt(%0)" ::"i"(N) : "memory"); }
__device__ __forceinline__ void wg_barrier() { asm volatile("s_waitcnt lgkmcnt(0)\n\ts_barrier" ::: "memory"); }

__device__ __forceinline__ f32x4 mfma(v4u a, v4u b, f32x4 c) {
  return __builtin_amdgcn_mfma_f32_16x16x32_bf16(__builtin_bit_cast(s16x8, a), __builtin_bit_cast(s16x8, b), c, 0, 0, 0);
}
__device__ __forceinline__ v4u tr_frag(LAS char* L, u32 a0, u32 a1) {
  const v2u x = __builtin_bit_cast(v2u, __builtin_amdgcn_ds_read_tr16_b64_v4i16((LAS s16x4*)(L + a0)));
  const v2u y = __builtin_bit_cast(v2u, __builtin_amdgcn_ds_read_tr16_b64_v4i16((LAS s16x4*)(L + a1)));
  return v4u{x[0], x[1], y[0], y[1]};
}
__device__ __forceinline__ v4u lds128(LAS char* L, u32 a) { return *(LAS v4u*)(L + a); }

__device__ __forceinline__ u32 lds_ld(LAS char* L, u32 a) { return *(volatile LAS u32*)(L + a); }
__device__ __forceinline__ void lds_st(LAS char* L, u32 a, u32 v) { *(volatile LAS u32*)(L + a) = v; }
__device__ __forceinline__ void lds_arrive(u32 lbase, u32 a) {
  const u32 addr = lbase + a, one = 1;
  asm volatile("s_waitcnt lgkmcnt(0)\n\ts_mov_b64 exec, 1\n\tds_add_u32 %0, %1\n\ts_mov_b64 exec, -1" ::"v"(addr), "v"(one) : "memory");
}
__device__ __forceinline__ void lds_flag(u32 lbase, u32 a) {
  const u32 addr = lbase + a, one = 1;
  asm volatile("s_waitcnt lgkmcnt(0)\n\ts_mov_b64 exec, 1\n\tds_write_b32 %0, %1\n\ts_mov_b64 exec, -1" ::"v"(addr), "v"(one) : "memory");
}
template <int SLEEP> __device__ __forceinline__ void lds_wait(LAS char* L, u32 a, u32 n) {
  while (lds_ld(L, a) < n) { if (SLEEP) __builtin_amdgcn_s_sleep(SLEEP); }
  asm volatile("" ::: "memory");
}

#define STAMP(k) do { } while (0)
#define STAMPS_OUT() do { } while (0)
#define SPINS(k, e) do { (void)(e); } while (0)

__global__ void __launch_bounds__(NTHR, 1) fused_decode_kernel(Params p) {
  __shared__ __attribute__((aligned(16))) char lds_raw[LDS_BYTES];
  LAS char* L = (LAS char*)lds_raw;
  const u32 lbase = (u32)(uintptr_t)L;
  const int tid = threadIdx.x, lane = tid & 63;
  const int wave = __builtin_amdgcn_readfirstlane(tid >> 6);
  const int wg = blockIdx.x, xx = wg & 7, j = wg >> 3;
  const int g = lane >> 4;
  asm volatile("" ::"s"(p.x), "s"(p.kc), "s"(p.vc), "s"(p.wq), "s"(p.wo), "s"(p.w1), "s"(p.w2), "s"(p.out), "s"(p.ws));
  STAMP(0);
  const int cs = j >> 2, ks = j & 3;
  const int db = j >> 3, ns = j & 7;
  const int hh = 4 * xx + db;
  const int bb = j >> 2, sp = j & 3;
  const bool ffn = xx < 7;
  const u32 trq = (u32)((lane >> 2) & 3), trp = (u32)(lane & 3);
  const u32 trf = ((trq >> 1) & 1) | (((u32)g & 1) << 1);
  const u32 tr_row = ((u32)g * 8 + trq) * 128 + trp * 8;

  const u32 wchunk = (u32)((lane & 7) ^ (((lane >> 4) & 1) << 1));
  const u32 wrow = (u32)(lane >> 3);
  const int fnt = hh & 3, fG = (hh >> 2) & 1, fcq = hh >> 3;
  const int fb = 4 * fG + 2 * (lane >> 5) + ((lane >> 4) & 1);
  const int fcol = ns * 64 + fnt * 16 + 4 * fcq + (lane & 3);
  v4u xv[2], code;
  v2u ev;
  u32 xres;
  {
    const int wl = (wave - 4) & 7;
    u64 pc;
    asm volatile("s_getpc_b64 %0" : "=s"(pc));
    const char* cb = (const char*)(pc & ~255ull);
    const int cp = j % CPF_N;
    const u32 co = (u32)((cp * 1024 < CPF_BYTES - 1024 ? cp * 1024 : CPF_BYTES - 1024) + lane * 16);
    const u32 xo = (u32)((wave & 3) * 2048 + lane * 16), zero = 0, ro = (u32)(fb * DM + fcol) * 2;
    const char* b0 = p.wq + ((size_t)(ks * 128 + 16 * wl) * 4096 + xx * 512 + cs * 64) * 2;
    const char* b1 = b0 + 8 * 8192;
    const u32 vo = wrow * 8192 + wchunk * 16, vo1 = vo ^ 64u;
    const u32 d0 = lbase + L_W + (2 * wl) * 1024, d1 = d0 + 1024;
    asm volatile(
        "s_cmp_lt_u32 %[wave], 4\n\ts_cselect_b64 exec, 0, -1\n\t"
        "s_mov_b32 m0, %[d0]\n\ts_nop 0\n\tglobal_load_lds_dwordx4 %[vo], %[b0] " WQ_POL "\n\t"
        "s_mov_b32 m0, %[d1]\n\ts_nop 0\n\tglobal_load_lds_dwordx4 %[vo1], %[b1] " WQ_POL "\n\t"
        "s_not_b64 exec, exec\n\t"
        "global_load_dwordx4 %[x0], %[xo], %[px]\n\t"
        "global_load_dwordx4 %[x1], %[xo], %[px] offset:1024\n\t"
        "global_load_dwordx2 %[ev], %[zero], %[ws] sc1\n\t"
        "global_load_dwordx4 %[code], %[co], %[cb]\n\t"
        "global_load_ushort %[xr], %[ro], %[px]\n\t"
        "s_mov_b64 exec, -1"
        : [x0] "=&v"(xv[0]), [x1] "=&v"(xv[1]), [ev] "=&v"(ev), [xr] "=&v"(xres), [code] "=&v"(code)
        : [wave] "s"(wave), [d0] "s"(d0), [d1] "s"(d1), [vo] "v"(vo), [vo1] "v"(vo1), [b0] "s"(b0), [b1] "s"(b1),
          [xo] "v"(xo), [px] "s"(p.x), [zero] "v"(zero), [ws] "s"(p.ws), [ro] "v"(ro), [co] "v"(co), [cb] "s"(cb)
        : "memory", "scc");
  }
  if (wave == 3 && lane < 32) lds_st(L, L_FLAG + lane * 4, 0u);
  wg_barrier();

  if (__builtin_expect(wave >= 4, LFIRST)) {
    __builtin_amdgcn_s_setprio(LPRIO);
    const int w = wave - 4;
    const size_t kvoff = ((((size_t)bb * HKV + xx) * SK + (SK - WINDOW) + sp * 256 + w * 32) * DH) * 2;
    const char* kb = p.kc + kvoff;
    const char* vb = p.vc + kvoff;
    const u32 r4 = (u32)(lane >> 4), c16 = (u32)(lane & 15);
    const u32 kvo = r4 * 256 + ((c16 ^ r4) * 16);
    const u32 vvo = r4 * 256 + ((c16 ^ (r4 << 1)) * 16);
    auto issueK = [&](int u) { const int pu = (u + j) & 7; glds<KV_NT>(kb + pu * 1024, kvo ^ (u32)((pu & 3) << 6), lbase + L_K + w * 8192 + pu * 1024); };
    auto issueV = [&](int u) { const int pu = (u + j) & 7; glds<KV_NT>(vb + pu * 1024, vvo ^ (u32)((pu & 1) << 7), lbase + L_V + w * 8192 + pu * 1024); };
    issueK(0); issueK(1); issueK(2);
    if (ffn) {
      const char* b1 = p.w1 + ((size_t)(ns * 64 + 8 * w) * FF + hh * 64) * 2;
      glds<W_NT>(b1, wrow * (FF * 2) + ((wchunk ^ ((u32)(w & 1) << 2)) * 16), lbase + L_W1 + w * 1024);
      waitvm<4>();
    } else {
      waitvm<3>();
    }
    lds_arrive(lbase, C_A);
    STAMP(1);
    issueK(3); waitvm<WIN>();
    issueK(4); waitvm<2>();
    lds_arrive(lbase, C_W1);
    issueK(5); waitvm<WIN>();
    issueK(6); waitvm<WIN>();
    issueK(7); waitvm<2>();
    if (w == 0) lds_flag(lbase, F_G);
    issueV(0); waitvm<WIN>();
    issueV(1); waitvm<2>();
    lds_flag(lbase, F_K + w * 4);
    STAMP(2);
    issueV(2); waitvm<WIN>();
    issueV(3); waitvm<WIN>();
    issueV(4); waitvm<WIN>();
    issueV(5); waitvm<WIN>();
    issueV(6); waitvm<WIN>();
    issueV(7);
    STAMP(3);
    lds_wait<1>(L, C_PP, 4);
    v4u P[8];
#pragma unroll
    for (int u = 0; u < 8; ++u) P[u] = lds128(L, L_K + u * 8192 + 4096 + g * 64 + (lane & 3) * 16);
    const f32x4 lsum = *(LAS f32x4*)(L + L_ML + (lane & 3) * 16);
    const float mx = *(LAS float*)(L + L_ML + 64 + (lane & 3) * 4);
    const v4u tv = lds128(L, L_TAG);
    const Tag tg{tv[0], tv[1], tv[2], tv[3]};
    const __amdgpu_buffer_rsrc_t rs = __builtin_amdgcn_make_buffer_rsrc((void*)p.ws, 0, (int)WS_END, 0x00020000);
    const u32 vrow = (u32)g * 4 + trq;
    u32 va = L_V + vrow * 256 + (((u32)w ^ (vrow & 7)) * 32) + trp * 8;
    asm volatile("" : "+v"(va));
    const int rec = (xx * 8 + bb) * 4 + sp;
    const bool mlane = w == 0 && g == 0 && (lane >> 2) == 1;
    const u32 ooff = mlane ? WS_ML + (rec * 4 + (lane & 3)) * 16 : WS_O + (rec * 128 + (4 * w + g) * 4 + (lane & 3)) * 16;
    STAMP(4);
    const char* wob = p.wo + ((size_t)((4 * xx + (w >> 1)) * 128 + 32 * db + 16 * (w & 1)) * DM + ns * 64) * 2;
    const char* w2b = p.w2 + ((size_t)((ffn ? hh : 0) * 64 + 8 * w) * DM + ns * 64) * 2;
    const char* wob1 = wob + 8 * 1024;
    u32 wvo = wrow * 1024 + wchunk * 16, wvo1 = wvo ^ 64u, wvo2 = wrow * 1024 + ((wchunk ^ ((u32)(w & 1) << 2)) * 16);
    asm volatile("" : "+v"(wvo), "+v"(wvo1), "+v"(wvo2) : "s"(wob), "s"(wob1), "s"(w2b));
    lds_wait<0>(L, C_P, 4);
    lds_wait<0>(L, C_H, 4);
    waitvm<0>();
    glds<W_NT>(wob, wvo, lbase + L_W + (2 * w) * 1024);
    glds<W_NT>(wob1, wvo1, lbase + L_W + (2 * w + 1) * 1024);
    if (ffn) glds<W_NT>(w2b, wvo2, lbase + L_W1 + w * 1024);
    STAMP(5);
    wg_barrier();
    STAMP(6);
    {
      v4u A[8];
#pragma unroll
      for (int u = 0; u < 8; ++u) A[u] = tr_frag(L, va + u * 8192, va + u * 8192 + 4096);
      __builtin_amdgcn_sched_barrier(0);
      f32x4 o = {0.f, 0.f, 0.f, 0.f};
#pragma unroll
      for (int u = 0; u < 8; ++u) o = mfma(A[u], P[u], o);
      if (mlane) o = f32x4{mx, (lsum[0] + lsum[1]) + (lsum[2] + lsum[3]), 0.f, 0.f};
      if ((lane & 15) < 4 || mlane) st_g(rs, ooff, pack_g(o, tg));
    }
    STAMP(7);
    if (w >= 4) {
      if (ffn) {
        const int gi = 32 * (w - 4) + (lane & 31);
        const u32 ho = WS_H + ((hh * 8 + 4 * (lane >> 5)) * 128 + gi) * 16;
        const u32 off[4] = {ho, ho + 2048, ho + 4096, ho + 6144};
        v4u ga[4];
        SPINS(12, (gather<4, 2>(rs, off, ga, tg)));
        const f32x4 a = (val_g(ga[0]) + val_g(ga[1])) + (val_g(ga[2]) + val_g(ga[3]));
        float r[4];
#pragma unroll
        for (int q = 0; q < 4; ++q) {
          auto sw = __builtin_amdgcn_permlane32_swap(f2u(a[q]), f2u(a[q]), false, false);
          const u32 s0 = sw[0], s1 = sw[1];
          const float hv = rbf(u2f(s0) + u2f(s1));
          r[q] = hv * __builtin_amdgcn_rcpf(1.f + __builtin_amdgcn_exp2f(-hv * 1.4426950408889634f));
        }
        if (lane < 32) *(LAS v2u*)(L + L_XH + (gi >> 4) * 160 + (gi & 15) * 8) = v2u{pk2(r[0], r[1]), pk2(r[2], r[3])};
        lds_arrive(lbase, C_HI);
      }
      waitvm<0>();
      lds_arrive(lbase, C_W2);
      STAMP(8);
    } else {
      waitvm<0>();
      lds_arrive(lbase, C_W2);
      lds_wait<1>(L, C_W2, WO_LATE == 1 ? 4 : 8);
      const int nt = w;
      const u32 a0 = tr_row + (((u32)nt ^ trf) * 32);
      v4u A[4];
#pragma unroll
      for (int t = 0; t < 4; ++t) A[t] = tr_frag(L, L_W + a0 + t * 4096, L_W + a0 + t * 4096 + 512);
      f32x4 acc = {0.f, 0.f, 0.f, 0.f};
      if (ffn) {
        v4u A2[2];
#pragma unroll
        for (int t = 0; t < 2; ++t) A2[t] = tr_frag(L, L_W1 + a0 + t * 4096, L_W1 + a0 + t * 4096 + 512);
        lds_wait<1>(L, C_HI, 4);
#pragma unroll
        for (int t = 0; t < 2; ++t) acc = mfma(lds128(L, L_XH + (lane & 7) * 160 + (4 * t + g) * 16), A2[t], acc);
      }
      u32 foff = WS_F + ((((ns * 4 + nt) * 2 + g) * 32 + hh) * 16 + (lane & 15)) * 16;
      u32 xqa = L_XQ + (lane & 7) * 288 + g * 16;
      asm volatile("" : "+v"(foff), "+v"(xqa));
      wg_barrier();
      STAMP(8);
      v4u B[4];
#pragma unroll
      for (int t = 0; t < 4; ++t) B[t] = lds128(L, xqa + t * 64);
      __builtin_amdgcn_sched_barrier(0);
#pragma unroll
      for (int t = 0; t < 4; ++t) acc = mfma(B[t], A[t], acc);
      if (lane < 32) st_g(rs, foff, pack_g(acc, tg));
      STAMP(9);
    }
    STAMPS_OUT();
    __builtin_amdgcn_s_waitcnt(0);
  } else {
    const int w = wave;
    const __amdgpu_buffer_rsrc_t rs = __builtin_amdgcn_make_buffer_rsrc((void*)p.ws, 0, (int)WS_END, 0x00020000);
    u32 tl0, tl1;
    {
      const char* tb = w == 0 ? p.ws
                     : w == 1 ? p.vc + ((((size_t)bb * HKV + xx) * SK + (SK - WINDOW) + sp * 256) * DH) * 2
                     : w == 2 ? p.wo + ((size_t)(4 * xx * 128 + 32 * db) * DM + ns * 64) * 2
                              : p.w2 + ((size_t)((ffn ? hh : 0) * 64) * DM + ns * 64) * 2;
      const u32 tstep = w == 0 ? 65536u : w == 2 ? 8192u : 4096u;
      const u32 tmax = w == 0 ? WS_END - 64 : w == 1 ? 65472u : w == 2 ? 3 * 131072u + 31 * 1024u : 63 * 1024u;
      const u32 to = min((u32)lane * tstep, tmax), zero = 0;
      asm volatile("global_load_dword %0, %2, %3\n\tglobal_load_dword %1, %4, %5" : "=&v"(tl0), "=&v"(tl1) : "v"(to), "s"(tb), "v"(zero), "s"(p.out) : "memory");
    }
    asm volatile("s_waitcnt vmcnt(5)" : "+v"(xv[0]), "+v"(xv[1])::"memory");
    STAMP(11);
    {
      float v[2][8], s1[2], s2[2], piv[2];
#pragma unroll
      for (int r = 0; r < 2; ++r) {
#pragma unroll
        for (int q = 0; q < 4; ++q) { v[r][2 * q] = bflo(xv[r][q]); v[r][2 * q + 1] = bfhi(xv[r][q]); }
        piv[r] = u2f(__builtin_amdgcn_readfirstlane(f2u(v[r][0])));
        float d[8];
#pragma unroll
        for (int q = 0; q < 8; ++q) d[q] = v[r][q] - piv[r];
        s1[r] = ((d[0] + d[1]) + (d[2] + d[3])) + ((d[4] + d[5]) + (d[6] + d[7]));
        s2[r] = fmaf(d[1], d[1], d[0] * d[0]) + fmaf(d[3], d[3], d[2] * d[2]) + (fmaf(d[5], d[5], d[4] * d[4]) + fmaf(d[7], d[7], d[6] * d[6]));
      }
#define RED4(C, RM) { const float a = dppz<C, RM>(s1[0]), b = dppz<C, RM>(s2[0]), c = dppz<C, RM>(s1[1]), d = dppz<C, RM>(s2[1]); s1[0] += a; s2[0] += b; s1[1] += c; s2[1] += d; }
      RED4(0x111, 0xf) RED4(0x112, 0xf) RED4(0x114, 0xf) RED4(0x118, 0xf) RED4(0x142, 0xa) RED4(0x143, 0xc)
#undef RED4
#pragma unroll
      for (int r = 0; r < 2; ++r) {
        const float t1 = u2f(__builtin_amdgcn_readlane(f2u(s1[r]), 63)), t2 = u2f(__builtin_amdgcn_readlane(f2u(s2[r]), 63));
        const float md = t1 * (1.f / DM);
        const float var = rbf(fmaxf(fmaf(-md, md, t2 * (1.f / DM)), 0.f));
        const float mean = rbf(piv[r] + md);
        const float den = rbf(__builtin_amdgcn_sqrtf(rbf(var + 1e-5f)));
        const float rd = __builtin_amdgcn_rcpf(den);
        v4u xn;
#pragma unroll
        for (int q = 0; q < 4; ++q) {
          const u32 d2 = pk2(v[r][2 * q] - mean, v[r][2 * q + 1] - mean);
          xn[q] = pk2(bflo(d2) * rd, bfhi(d2) * rd);
        }
        if ((lane >> 4) == ks) *(LAS v4u*)(L + L_XQ + (2 * w + r) * 288 + (lane & 15) * 16) = xn;
        if ((lane >> 3) == ns) *(LAS v4u*)(L + L_XH + (2 * w + r) * 160 + (lane & 7) * 16) = xn;
      }
    }
    asm volatile("s_waitcnt vmcnt(4)" : "+v"(ev)::"memory");
    const u64 e = (((u64)(u32)__builtin_amdgcn_readfirstlane(ev[1]) << 32) | (u32)__builtin_amdgcn_readfirstlane(ev[0])) + 1;
    const Tag tg = make_tag(e);
    if (w == 0 && lane == 0) *(LAS v4u*)(L + L_TAG) = v4u{tg.t0, tg.t1, tg.t2, tg.t3};
    lds_arrive(lbase, C_A);
    STAMP(1);
    LAS char* T = L + L_TR + w * 512;
    const int tb = (lane >> 2) & 7, tG = lane & 3;
    const u32 a0 = tr_row + (((u32)w ^ trf) * 32);
    lds_wait<1>(L, C_A, 12);
    STAMP(2);
    {
      v4u A[4], B[4];
#pragma unroll
      for (int t = 0; t < 4; ++t) { A[t] = tr_frag(L, L_W + a0 + t * 4096, L_W + a0 + t * 4096 + 512); B[t] = lds128(L, L_XQ + (lane & 7) * 288 + (4 * t + g) * 16); }
      __builtin_amdgcn_sched_barrier(0);
      f32x4 acc = {0.f, 0.f, 0.f, 0.f};
#pragma unroll
      for (int t = 0; t < 4; ++t) acc = mfma(A[t], B[t], acc);
      if ((lane & 15) < 8) *(LAS f32x4*)(T + (lane & 7) * 64 + g * 16) = acc;
      const f32x4 tq = *(LAS f32x4*)(T + (lane & 31) * 16);
      if (lane < 32) st_g(rs, WS_Q + (((xx * 8 + tb) * 4 + ks) * 128 + cs * 16 + w * 4 + tG) * 16, pack_g(tq, tg));
    }
    lds_arrive(lbase, C_P);
    STAMP(3);
    if (ffn) {
      lds_wait<1>(L, C_W1, 8);
      v4u A[2], B[2];
#pragma unroll
      for (int t = 0; t < 2; ++t) { A[t] = tr_frag(L, L_W1 + a0 + t * 4096, L_W1 + a0 + t * 4096 + 512); B[t] = lds128(L, L_XH + (lane & 7) * 160 + (4 * t + g) * 16); }
      f32x4 acc = {0.f, 0.f, 0.f, 0.f};
#pragma unroll
      for (int t = 0; t < 2; ++t) acc = mfma(A[t], B[t], acc);
      if ((lane & 15) < 8) *(LAS f32x4*)(T + (lane & 7) * 64 + g * 16) = acc;
      const f32x4 th = *(LAS f32x4*)(T + (lane & 31) * 16);
      if (lane < 32) st_g(rs, WS_H + (((hh * 8 + ns) * 8 + tb) * 16 + w * 4 + tG) * 16, pack_g(th, tg));
    }
    lds_arrive(lbase, C_H);
    asm volatile("s_waitcnt vmcnt(0)" : "+v"(xres), "+v"(code), "+v"(tl0), "+v"(tl1)::"memory");
    if (w < 2) {
      lds_wait<2>(L, F_G, 1);
      const u32 qo = WS_Q + ((xx * 8 + bb) * 4 * 128 + w * 64 + lane) * 16;
      const u32 off[4] = {qo, qo + 2048, qo + 4096, qo + 6144};
      v4u ga[4];
      SPINS(12, (gather<4, GSLEEP>(rs, off, ga, tg)));
      const f32x4 a = (val_g(ga[0]) + val_g(ga[1])) + (val_g(ga[2]) + val_g(ga[3]));
      *(LAS v2u*)(L + L_QI + (2 * w + (lane >> 5)) * 288 + (lane & 31) * 8) = v2u{pk2(a[0], a[1]), pk2(a[2], a[3])};
      lds_arrive(lbase, C_Q);
    }
    STAMP(4);
    lds_wait<1>(L, C_Q, 2);
    lds_wait<1>(L, F_K + (2 * w) * 4, 1);
    lds_wait<1>(L, F_K + (2 * w + 1) * 4, 1);
    STAMP(5);
    {
      const u32 c16 = (u32)(lane & 15);
      v4u B[4], A0[2][4], A1[2][4];
#pragma unroll
      for (int t = 0; t < 4; ++t) B[t] = lds128(L, L_QI + (lane & 3) * 288 + (4 * t + g) * 16);
#pragma unroll
      for (int u = 0; u < 2; ++u) {
        const u32 ka = L_K + (2 * w + u) * 8192 + c16 * 256 + (((u32)g ^ c16) * 16);
#pragma unroll
        for (int t = 0; t < 4; ++t) { A0[u][t] = lds128(L, ka ^ (u32)(t << 6)); A1[u][t] = lds128(L, (ka ^ (u32)(t << 6)) + 4096); }
      }
      __builtin_amdgcn_sched_barrier(0);
      f32x4 sc[4];
#pragma unroll
      for (int u = 0; u < 4; ++u) sc[u] = f32x4{0.f, 0.f, 0.f, 0.f};
#pragma unroll
      for (int t = 0; t < 4; ++t) {
        sc[0] = mfma(A0[0][t], B[t], sc[0]); sc[1] = mfma(A1[0][t], B[t], sc[1]);
        sc[2] = mfma(A0[1][t], B[t], sc[2]); sc[3] = mfma(A1[1][t], B[t], sc[3]);
      }
      float s[16];
#pragma unroll
      for (int u = 0; u < 4; ++u) {
        const u32 a = pk2(sc[u][0], sc[u][1]), b = pk2(sc[u][2], sc[u][3]);
        const u32 c = pk2(bflo(a) * 0.08838834764831845f, bfhi(a) * 0.08838834764831845f);
        const u32 d = pk2(bflo(b) * 0.08838834764831845f, bfhi(b) * 0.08838834764831845f);
        s[4 * u] = bflo(c); s[4 * u + 1] = bfhi(c); s[4 * u + 2] = bflo(d); s[4 * u + 3] = bfhi(d);
      }
      float m = fmaxf(fmaxf(fmaxf(fmaxf(s[0], s[1]), fmaxf(s[2], s[3])), fmaxf(fmaxf(s[4], s[5]), fmaxf(s[6], s[7]))),
                      fmaxf(fmaxf(fmaxf(s[8], s[9]), fmaxf(s[10], s[11])), fmaxf(fmaxf(s[12], s[13]), fmaxf(s[14], s[15]))));
      m = x16_32_max(m);
      if (lane < 4) *(LAS float*)(L + L_MX + (lane * 4 + w) * 4) = m;
      lds_arrive(lbase, C_M);
      lds_wait<0>(L, C_M, 4);
      const f32x4 m0 = *(LAS f32x4*)(L + L_MX + (lane & 3) * 16);
      const float M = fmaxf(fmaxf(m0[0], m0[1]), fmaxf(m0[2], m0[3]));
      const float Ml = M * 1.4426950408889634f;
      float ex[16];
#pragma unroll
      for (int r = 0; r < 16; ++r) ex[r] = __builtin_amdgcn_exp2f(fmaf(s[r], 1.4426950408889634f, -Ml));
      float l = (((ex[0] + ex[1]) + (ex[2] + ex[3])) + ((ex[4] + ex[5]) + (ex[6] + ex[7]))) + (((ex[8] + ex[9]) + (ex[10] + ex[11])) + ((ex[12] + ex[13]) + (ex[14] + ex[15])));
      if ((lane & 15) < 4) {
#pragma unroll
        for (int u = 0; u < 2; ++u)
          *(LAS v4u*)(L + L_K + (2 * w + u) * 8192 + 4096 + g * 64 + (lane & 3) * 16) =
              v4u{pk2(ex[8 * u], ex[8 * u + 1]), pk2(ex[8 * u + 2], ex[8 * u + 3]), pk2(ex[8 * u + 4], ex[8 * u + 5]), pk2(ex[8 * u + 6], ex[8 * u + 7])};
      }
      l = x16_32_sum(l);
      if (lane < 4) { *(LAS float*)(L + L_ML + (lane * 4 + w) * 4) = l; if (w == 0) *(LAS float*)(L + L_ML + 64 + lane * 4) = M; }
      lds_arrive(lbase, C_PP);
    }
    STAMP(6);
    wg_barrier();
    STAMP(7);
    {
      const u32 oo = WS_O + (((xx * 8 + 2 * w + (lane >> 5)) * 4) * 128 + db * 32 + (lane & 31)) * 16;
      const u32 off[4] = {oo, oo + 2048, oo + 4096, oo + 6144};
      v4u go[4];
      if (w == 0) {
        const u32 mo = WS_ML + (xx * 128 + lane) * 16;
        const u32 moff[2] = {mo, mo + 1024};
        v4u gm[2];
        __builtin_amdgcn_s_sleep(MSLEEP0);
        SPINS(13, (gather<2, MSLEEP>(rs, moff, gm, tg)));
        lds_flag(lbase, F_ML);
#pragma unroll
        for (int s = 0; s < 4; ++s) go[s] = ld_g(rs, off[s]);
#pragma unroll
        for (int r = 0; r < 2; ++r) {
          const float ms = u2f(gm[r][0] & 0xfffff000u), ls = u2f(gm[r][1] & 0xfffff000u);
          float M = fmaxf(ms, dppm<0x124>(ms));
          M = fmaxf(M, dppm<0x128>(M));
          const float ew = __builtin_amdgcn_exp2f((ms - M) * 1.4426950408889634f);
          float Ls = ew * ls;
          Ls += dppm<0x124>(Ls);
          Ls += dppm<0x128>(Ls);
          *(LAS float*)(L + L_WT + (((4 * r + g) * 4 + (lane & 3)) * 4 + ((lane >> 2) & 3)) * 4) = ew * __builtin_amdgcn_rcpf(Ls);
        }
        lds_arrive(lbase, F_ML);
      } else {
        lds_wait<0>(L, F_ML, 1);
#pragma unroll
        for (int s = 0; s < 4; ++s) go[s] = ld_g(rs, off[s]);
      }
      STAMP(8);
      {
        u32 acc0 = chk(chk(0u, go[0], tg), go[1], tg), acc1 = chk(chk(0u, go[2], tg), go[3], tg);
        if (!__all(((acc0 | acc1) & 0xfffu) == 0u)) (void)gather<4, 0>(rs, off, go, tg);
      }
      lds_wait<0>(L, F_ML, 2);
      const f32x4 wt = *(LAS f32x4*)(L + L_WT + ((2 * w + (lane >> 5)) * 4 + (lane & 3)) * 16);
      const f32x4 O = (val_g(go[0]) * wt[0] + val_g(go[1]) * wt[1]) + (val_g(go[2]) * wt[2] + val_g(go[3]) * wt[3]);
      *(LAS v2u*)(L + L_XQ + (2 * w + (lane >> 5)) * 288 + ((lane & 3) * 32 + ((lane >> 2) & 7) * 4) * 2) = v2u{pk2(O[0], O[1]), pk2(O[2], O[3])};
      STAMP(9);
      wg_barrier();
    }
    if (w == 0) {
      const u32 fo = WS_F + ((((ns * 4 + fnt) * 2 + fG) * 32 + (lane >> 2)) * 16 + 4 * fcq + (lane & 3)) * 16;
      const u32 off[2] = {fo, fo + 16 * 256};
      v4u gg[2];
      __builtin_amdgcn_s_sleep(FSLEEP0);
      SPINS(14, (gather<2, FSLEEP>(rs, off, gg, tg)));
      const f32x4 v = val_g(gg[0]) + val_g(gg[1]);
      auto a02 = __builtin_amdgcn_permlane32_swap(f2u(v[0]), f2u(v[2]), false, false);
      auto a13 = __builtin_amdgcn_permlane32_swap(f2u(v[1]), f2u(v[3]), false, false);
      const u32 p0 = a02[0], p1 = a02[1], p2 = a13[0], p3 = a13[1];
      const float lo = u2f(p0) + u2f(p1), hi = u2f(p2) + u2f(p3);
      auto a = __builtin_amdgcn_permlane16_swap(f2u(lo), f2u(hi), false, false);
      const u32 q0 = a[0], q1 = a[1];
      float t = u2f(q0) + u2f(q1);
      t += dppm<0x124>(t);
      t += dppm<0x128>(t);
      const u32 ob = pk2(bflo(xres) + t, 0.f);
      if ((lane & 12) == 0) {
        *(GAS unsigned short*)(p.out + (fb * DM + fcol) * 2) = (unsigned short)ob;
      }
      if (wg == 0 && lane == 0) __hip_atomic_store((GAS u64*)(p.ws + WS_EPOCH), e, __ATOMIC_RELAXED, __HIP_MEMORY_SCOPE_AGENT);
      STAMP(15);
    }
    STAMPS_OUT();
    __builtin_amdgcn_s_waitcnt(0);
  }
}

}

extern "C" void kernel_launch(void* const* d_in, const int* in_sizes, int n_in, void* d_out, int out_size, void* d_ws,
                              size_t ws_size, hipStream_t stream) {
  (void)in_sizes; (void)n_in; (void)out_size; (void)ws_size;
  Params p;
  p.x = (const char*)d_in[0];
  p.kc = (const char*)d_in[1];
  p.vc = (const char*)d_in[2];
  p.wq = (const char*)d_in[3];
  p.wo = (const char*)d_in[4];
  p.w1 = (const char*)d_in[6];
  p.w2 = (const char*)d_in[7];
  p.out = (char*)d_out;
  p.ws = (char*)d_ws;
  hipLaunchKernelGGL(fused_decode_kernel, dim3(NWG), dim3(NTHR), 0, stream, p);
}
```

```cpp
#include <hip/hip_runtime.h>
#include <stdint.h>

#define GAS __attribute__((address_space(1)))
#define LAS __attribute__((address_space(3)))
typedef unsigned u32;
typedef unsigned long long u64;
typedef u32 v4u __attribute__((ext_vector_type(4)));
typedef u32 v2u __attribute__((ext_vector_type(2)));
typedef float f32x4 __attribute__((ext_vector_type(4)));
typedef float f32x2 __attribute__((ext_vector_type(2)));
typedef short s16x8 __attribute__((ext_vector_type(8)));
typedef short s16x4 __attribute__((ext_vector_type(4)));
typedef __bf16 bf16x2 __attribute__((ext_vector_type(2)));

#define KV_NT 1
#define W_NT 1
#define WIN 2
#define GSLEEP 6
#define LPRIO 3
#define FSLEEP0 20
#define FSLEEP 1
#define MSLEEP0 16
#define MSLEEP 1
#define WO_LATE 2
#define LFIRST 1
#define CPF 1
#define CPF_BACK 0
#define CPF_N 12
#define CPF_BYTES 11520
#define WQ_POL "nt"
#define TLBW 1
#define OUT_SC1 0

namespace {

constexpr int DM = 512, HKV = 8, SK = 4096, DH = 128, WINDOW = 1024, FF = 1792;
constexpr int NWG = 256, NTHR = 768;

constexpr u32 WS_EPOCH = 0;
constexpr u32 WS_Q = 4096;
constexpr u32 WS_H = WS_Q + 8 * 8 * 4 * 128 * 16;
constexpr u32 WS_O = WS_H + 28 * 8 * 8 * 16 * 16;
constexpr u32 WS_ML = WS_O + 8 * 8 * 4 * 128 * 16;
constexpr u32 WS_F = WS_ML + 8 * 8 * 4 * 4 * 16;
constexpr u32 WS_END = WS_F + 8 * 4 * 2 * 32 * 16 * 16;
constexpr u32 WS_ST = WS_END;

constexpr u32 L_K = 0, L_V = 65536, L_W = 131072, L_W1 = L_W + 16384;
constexpr u32 L_XQ = L_W + 24576;
constexpr u32 L_XH = L_XQ + 8 * 288;
constexpr u32 L_QI = L_XH + 8 * 160;
constexpr u32 L_TR = L_QI + 4 * 288;
constexpr u32 L_ML = L_TR + 2048;
constexpr u32 L_MX = L_ML + 96;
constexpr u32 L_WT = L_MX + 64;
constexpr u32 L_FLAG = L_WT + 512;
constexpr u32 L_END = L_FLAG + 128;
static_assert(L_END <= 163840 && L_ML % 16 == 0 && L_MX % 16 == 0 && L_WT % 16 == 0 && L_FLAG % 16 == 0, "LDS");
constexpr u32 LDS_BYTES = 163840;
constexpr u32 C_A = L_FLAG, C_P = L_FLAG + 4, C_Q = L_FLAG + 8, C_M = L_FLAG + 12, C_W2 = L_FLAG + 16, C_W1 = L_FLAG + 20, C_H = L_FLAG + 24,
              C_PP = L_FLAG + 28, F_G = L_FLAG + 32, F_ML = L_FLAG + 36, C_HI = L_FLAG + 40;
constexpr u32 F_K = L_FLAG + 64;
constexpr u32 L_TAG = L_FLAG + 96;

constexpr u32 SPIN_CAP = 1u << 16;

struct Params {
  const char* x; const char* kc; const char* vc; const char* wq; const char* wo; const char* w1; const char* w2;
  char* out; char* ws;
};

__device__ __forceinline__ float u2f(u32 u) { return __builtin_bit_cast(float, u); }
__device__ __forceinline__ u32 f2u(float f) { return __builtin_bit_cast(u32, f); }
__device__ __forceinline__ float bflo(u32 u) { return u2f(u << 16); }
__device__ __forceinline__ float bfhi(u32 u) { return u2f(u & 0xffff0000u); }
__device__ __forceinline__ u32 pk2(float lo, float hi) {
  f32x2 v = {lo, hi};
  return __builtin_bit_cast(u32, __builtin_convertvector(v, bf16x2));
}
__device__ __forceinline__ float rbf(float f) { return bflo(pk2(f, f) & 0xffffu); }

template <int CTRL, int RM> __device__ __forceinline__ float dppz(float v) {
  return u2f((u32)__builtin_amdgcn_update_dpp(0, (int)f2u(v), CTRL, RM, 0xf, true));
}
template <int CTRL> __device__ __forceinline__ float dppm(float v) {
  return u2f((u32)__builtin_amdgcn_mov_dpp((int)f2u(v), CTRL, 0xf, 0xf, true));
}
__device__ __forceinline__ float x16_32_max(float x) {
  auto s = __builtin_amdgcn_permlane16_swap(f2u(x), f2u(x), false, false);
  const u32 s0 = s[0], s1 = s[1];
  x = fmaxf(u2f(s0), u2f(s1));
  auto t = __builtin_amdgcn_permlane32_swap(f2u(x), f2u(x), false, false);
  const u32 t0 = t[0], t1 = t[1];
  return fmaxf(u2f(t0), u2f(t1));
}
__device__ __forceinline__ float x16_32_sum(float x) {
  auto s = __builtin_amdgcn_permlane16_swap(f2u(x), f2u(x), false, false);
  const u32 s0 = s[0], s1 = s[1];
  x = u2f(s0) + u2f(s1);
  auto t = __builtin_amdgcn_permlane32_swap(f2u(x), f2u(x), false, false);
  const u32 t0 = t[0], t1 = t[1];
  return u2f(t0) + u2f(t1);
}

struct Tag { u32 t0, t1, t2, t3; };
__device__ __forceinline__ Tag make_tag(u64 e) {
  Tag t; t.t0 = (u32)e & 0xfffu; t.t1 = (u32)(e >> 12) & 0xfffu; t.t2 = t.t0; t.t3 = (u32)(e >> 24) & 0xfffu; return t;
}
__device__ __forceinline__ u32 pkw(float f, u32 t) { return ((f2u(f) + 0x800u) & 0xfffff000u) | t; }
__device__ __forceinline__ v4u pack_g(f32x4 v, const Tag& t) { return v4u{pkw(v[0], t.t0), pkw(v[1], t.t1), pkw(v[2], t.t2), pkw(v[3], t.t3)}; }
__device__ __forceinline__ f32x4 val_g(v4u g) {
  return f32x4{u2f(g[0] & 0xfffff000u), u2f(g[1] & 0xfffff000u), u2f(g[2] & 0xfffff000u), u2f(g[3] & 0xfffff000u)};
}
__device__ __forceinline__ u32 chk(u32 acc, v4u g, const Tag& t) {
  acc = __builtin_amdgcn_bitop3_b32(acc, g[0], t.t0, 0xF6);
  acc = __builtin_amdgcn_bitop3_b32(acc, g[1], t.t1, 0xF6);
  acc = __builtin_amdgcn_bitop3_b32(acc, g[2], t.t2, 0xF6);
  acc = __builtin_amdgcn_bitop3_b32(acc, g[3], t.t3, 0xF6);
  return acc;
}
constexpr int AUX_SC1 = 16;
__device__ __forceinline__ v4u ld_g(__amdgpu_buffer_rsrc_t rs, u32 off) { return __builtin_amdgcn_raw_buffer_load_b128(rs, off, 0, AUX_SC1); }
__device__ __forceinline__ void st_g(__amdgpu_buffer_rsrc_t rs, u32 off, v4u v) { __builtin_amdgcn_raw_buffer_store_b128(v, rs, off, 0, AUX_SC1); }
template <int N, int SLEEP> __device__ __forceinline__ u32 gather(__amdgpu_buffer_rsrc_t rs, const u32 (&off)[N], v4u (&g)[N], const Tag& tg) {
  u32 spin = 0;
  while (true) {
#pragma unroll
    for (int i = 0; i < N; ++i) g[i] = ld_g(rs, off[i]);
    u32 acc0 = 0, acc1 = 0;
#pragma unroll
    for (int i = 0; i < N; ++i) { if (i & 1) acc1 = chk(acc1, g[i], tg); else acc0 = chk(acc0, g[i], tg); }
    if (__all(((acc0 | acc1) & 0xfffu) == 0u) || ++spin > SPIN_CAP) break;
    if (SLEEP) __builtin_amdgcn_s_sleep(SLEEP);
    asm volatile("" ::: "memory");
  }
  return spin;
}

template <int NT> __device__ __forceinline__ void glds(const char* sbase, u32 voff, u32 dst) {
  if constexpr (NT)
    asm volatile("s_mov_b32 m0, %2\n\ts_nop 0\n\tglobal_load_lds_dwordx4 %0, %1 nt" ::"v"(voff), "s"(sbase), "s"(dst) : "memory");
  else
    asm volatile("s_mov_b32 m0, %2\n\ts_nop 0\n\tglobal_load_lds_dwordx4 %0, %1" ::"v"(voff), "s"(sbase), "s"(dst) : "memory");
}
template <int N> __device__ __forceinline__ void waitvm() { asm volatile("s_waitcnt vmcnt(%0)" ::"i"(N) : "memory"); }
__device__ __forceinline__ void wg_barrier() { asm volatile("s_waitcnt lgkmcnt(0)\n\ts_barrier" ::: "memory"); }

__device__ __forceinline__ f32x4 mfma(v4u a, v4u b, f32x4 c) {
  return __builtin_amdgcn_mfma_f32_16x16x32_bf16(__builtin_bit_cast(s16x8, a), __builtin_bit_cast(s16x8, b), c, 0, 0, 0);
}
__device__ __forceinline__ v4u tr_frag(LAS char* L, u32 a0, u32 a1) {
  const v2u x = __builtin_bit_cast(v2u, __builtin_amdgcn_ds_read_tr16_b64_v4i16((LAS s16x4*)(L + a0)));
  const v2u y = __builtin_bit_cast(v2u, __builtin_amdgcn_ds_read_tr16_b64_v4i16((LAS s16x4*)(L + a1)));
  return v4u{x[0], x[1], y[0], y[1]};
}
__device__ __forceinline__ v4u lds128(LAS char* L, u32 a) { return *(LAS v4u*)(L + a); }

__device__ __forceinline__ u32 lds_ld(LAS char* L, u32 a) { return *(volatile LAS u32*)(L + a); }
__device__ __forceinline__ void lds_st(LAS char* L, u32 a, u32 v) { *(volatile LAS u32*)(L + a) = v; }
__device__ __forceinline__ void lds_arrive(u32 lbase, u32 a) {
  const u32 addr = lbase + a, one = 1;
  asm volatile("s_waitcnt lgkmcnt(0)\n\ts_mov_b64 exec, 1\n\tds_add_u32 %0, %1\n\ts_mov_b64 exec, -1" ::"v"(addr), "v"(one) : "memory");
}
__device__ __forceinline__ void lds_flag(u32 lbase, u32 a) {
  const u32 addr = lbase + a, one = 1;
  asm volatile("s_waitcnt lgkmcnt(0)\n\ts_mov_b64 exec, 1\n\tds_write_b32 %0, %1\n\ts_mov_b64 exec, -1" ::"v"(addr), "v"(one) : "memory");
}
template <int SLEEP> __device__ __forceinline__ void lds_wait(LAS char* L, u32 a, u32 n) {
  while (lds_ld(L, a) < n) { if (SLEEP) __builtin_amdgcn_s_sleep(SLEEP); }
  asm volatile("" ::: "memory");
}

#define STAMP(k) do { } while (0)
#define STAMPS_OUT() do { } while (0)
#define SPINS(k, e) do { (void)(e); } while (0)

__global__ void __launch_bounds__(NTHR, 1) fused_decode_kernel(Params p) {
  __shared__ __attribute__((aligned(16))) char lds_raw[LDS_BYTES];
  LAS char* L = (LAS char*)lds_raw;
  const u32 lbase = (u32)(uintptr_t)L;
  const int tid = threadIdx.x, lane = tid & 63;
  const int wave = __builtin_amdgcn_readfirstlane(tid >> 6);
  const int wg = blockIdx.x, xx = wg & 7, j = wg >> 3;
  const int g = lane >> 4;
  asm volatile("" ::"s"(p.x), "s"(p.kc), "s"(p.vc), "s"(p.wq), "s"(p.wo), "s"(p.w1), "s"(p.w2), "s"(p.out), "s"(p.ws));
  STAMP(0);
  const int cs = j >> 2, ks = j & 3;
  const int db = j >> 3, ns = j & 7;
  const int hh = 4 * xx + db;
  const int bb = j >> 2, sp = j & 3;
  const bool ffn = xx < 7;
  const u32 trq = (u32)((lane >> 2) & 3), trp = (u32)(lane & 3);
  const u32 trf = ((trq >> 1) & 1) | (((u32)g & 1) << 1);
  const u32 tr_row = ((u32)g * 8 + trq) * 128 + trp * 8;

  const u32 wchunk = (u32)((lane & 7) ^ (((lane >> 4) & 1) << 1));
  const u32 wrow = (u32)(lane >> 3);
  const int fnt = hh & 3, fG = (hh >> 2) & 1, fcq = hh >> 3;
  const int fb = 4 * fG + 2 * (lane >> 5) + ((lane >> 4) & 1);
  const int fcol = ns * 64 + fnt * 16 + 4 * fcq + (lane & 3);
  v4u xv[2], code;
  v2u ev;
  u32 xres;
  {
    const int wl = (wave - 4) & 7;
    u64 pc;
    asm volatile("s_getpc_b64 %0" : "=s"(pc));
    const char* cb = (const char*)(pc & ~255ull);
    const int cp = j % CPF_N;
    const u32 co = (u32)((cp * 1024 < CPF_BYTES - 1024 ? cp * 1024 : CPF_BYTES - 1024) + lane * 16);
    const u32 xo = (u32)((wave & 3) * 2048 + lane * 16), zero = 0, ro = (u32)(fb * DM + fcol) * 2;
    const char* b0 = p.wq + ((size_t)(ks * 128 + 16 * wl) * 4096 + xx * 512 + cs * 64) * 2;
    const char* b1 = b0 + 8 * 8192;
    const u32 vo = wrow * 8192 + wchunk * 16, vo1 = vo ^ 64u;
    const u32 d0 = lbase + L_W + (2 * wl) * 1024, d1 = d0 + 1024;
    asm volatile(
        "s_cmp_lt_u32 %[wave], 4\n\ts_cselect_b64 exec, 0, -1\n\t"
        "s_mov_b32 m0, %[d0]\n\ts_nop 0\n\tglobal_load_lds_dwordx4 %[vo], %[b0] " WQ_POL "\n\t"
        "s_mov_b32 m0, %[d1]\n\ts_nop 0\n\tglobal_load_lds_dwordx4 %[vo1], %[b1] " WQ_POL "\n\t"
        "s_not_b64 exec, exec\n\t"
        "global_load_dwordx4 %[x0], %[xo], %[px]\n\t"
        "global_load_dwordx4 %[x1], %[xo], %[px] offset:1024\n\t"
        "global_load_dwordx2 %[ev], %[zero], %[ws] sc1\n\t"
        "global_load_dwordx4 %[code], %[co], %[cb]\n\t"
        "global_load_ushort %[xr], %[ro], %[px]\n\t"
        "s_mov_b64 exec, -1"
        : [x0] "=&v"(xv[0]), [x1] "=&v"(xv[1]), [ev] "=&v"(ev), [xr] "=&v"(xres), [code] "=&v"(code)
        : [wave] "s"(wave), [d0] "s"(d0), [d1] "s"(d1), [vo] "v"(vo), [vo1] "v"(vo1), [b0] "s"(b0), [b1] "s"(b1),
          [xo] "v"(xo), [px] "s"(p.x), [zero] "v"(zero), [ws] "s"(p.ws), [ro] "v"(ro), [co] "v"(co), [cb] "s"(cb)
        : "memory", "scc");
  }
  if (wave == 3 && lane < 32) lds_st(L, L_FLAG + lane * 4, 0u);
  wg_barrier();

  if (__builtin_expect(wave >= 4, LFIRST)) {
    __builtin_amdgcn_s_setprio(LPRIO);
    const int w = wave - 4;
    const size_t kvoff = ((((size_t)bb * HKV + xx) * SK + (SK - WINDOW) + sp * 256 + w * 32) * DH) * 2;
    const char* kb = p.kc + kvoff;
    const char* vb = p.vc + kvoff;
    const u32 r4 = (u32)(lane >> 4), c16 = (u32)(lane & 15);
    const u32 kvo = r4 * 256 + ((c16 ^ r4) * 16);
    const u32 vvo = r4 * 256 + ((c16 ^ (r4 << 1)) * 16);
    auto issueK = [&](int u) { const int pu = (u + j) & 7; glds<KV_NT>(kb + pu * 1024, kvo ^ (u32)((pu & 3) << 6), lbase + L_K + w * 8192 + pu * 1024); };
    auto issueV = [&](int u) { const int pu = (u + j) & 7; glds<KV_NT>(vb + pu * 1024, vvo ^ (u32)((pu & 1) << 7), lbase + L_V + w * 8192 + pu * 1024); };
    issueK(0); issueK(1); issueK(2);
    if (ffn) {
      const char* b1 = p.w1 + ((size_t)(ns * 64 + 8 * w) * FF + hh * 64) * 2;
      glds<W_NT>(b1, wrow * (FF * 2) + ((wchunk ^ ((u32)(w & 1) << 2)) * 16), lbase + L_W1 + w * 1024);
      waitvm<4>();
    } else {
      waitvm<3>();
    }
    lds_arrive(lbase, C_A);
    STAMP(1);
    issueK(3); waitvm<WIN>();
    issueK(4); waitvm<2>();
    lds_arrive(lbase, C_W1);
    issueK(5); waitvm<WIN>();
    issueK(6); waitvm<WIN>();
    issueK(7); waitvm<2>();
    if (w == 0) lds_flag(lbase, F_G);
    issueV(0); waitvm<WIN>();
    issueV(1); waitvm<2>();
    lds_flag(lbase, F_K + w * 4);
    STAMP(2);
    issueV(2); waitvm<WIN>();
    issueV(3); waitvm<WIN>();
    issueV(4); waitvm<WIN>();
    issueV(5); waitvm<WIN>();
    issueV(6); waitvm<WIN>();
    issueV(7);
    STAMP(3);
    lds_wait<1>(L, C_PP, 4);
    v4u P[8];
#pragma unroll
    for (int u = 0; u < 8; ++u) P[u] = lds128(L, L_K + u * 8192 + 4096 + g * 64 + (lane & 3) * 16);
    const f32x4 lsum = *(LAS f32x4*)(L + L_ML + (lane & 3) * 16);
    const float mx = *(LAS float*)(L + L_ML + 64 + (lane & 3) * 4);
    const v4u tv = lds128(L, L_TAG);
    const Tag tg{tv[0], tv[1], tv[2], tv[3]};
    const __amdgpu_buffer_rsrc_t rs = __builtin_amdgcn_make_buffer_rsrc((void*)p.ws, 0, (int)WS_END, 0x00020000);
    const u32 vrow = (u32)g * 4 + trq;
    u32 va = L_V + vrow * 256 + (((u32)w ^ (vrow & 7)) * 32) + trp * 8;
    asm volatile("" : "+v"(va));
    const int rec = (xx * 8 + bb) * 4 + sp;
    const bool mlane = w == 0 && g == 0 && (lane >> 2) == 1;
    const u32 ooff = mlane ? WS_ML + (rec * 4 + (lane & 3)) * 16 : WS_O + (rec * 128 + (4 * w + g) * 4 + (lane & 3)) * 16;
    STAMP(4);
    const char* wob = p.wo + ((size_t)((4 * xx + (w >> 1)) * 128 + 32 * db + 16 * (w & 1)) * DM + ns * 64) * 2;
    const char* w2b = p.w2 + ((size_t)((ffn ? hh : 0) * 64 + 8 * w) * DM + ns * 64) * 2;
    const char* wob1 = wob + 8 * 1024;
    u32 wvo = wrow * 1024 + wchunk * 16, wvo1 = wvo ^ 64u, wvo2 = wrow * 1024 + ((wchunk ^ ((u32)(w & 1) << 2)) * 16);
    asm volatile("" : "+v"(wvo), "+v"(wvo1), "+v"(wvo2) : "s"(wob), "s"(wob1), "s"(w2b));
    lds_wait<0>(L, C_P, 4);
    lds_wait<0>(L, C_H, 4);
    waitvm<0>();
    glds<W_NT>(wob, wvo, lbase + L_W + (2 * w) * 1024);
    glds<W_NT>(wob1, wvo1, lbase + L_W + (2 * w + 1) * 1024);
    if (ffn) glds<W_NT>(w2b, wvo2, lbase + L_W1 + w * 1024);
    STAMP(5);
    wg_barrier();
    STAMP(6);
    {
      v4u A[8];
#pragma unroll
      for (int u = 0; u < 8; ++u) A[u] = tr_frag(L, va + u * 8192, va + u * 8192 + 4096);
      __builtin_amdgcn_sched_barrier(0);
      f32x4 o = {0.f, 0.f, 0.f, 0.f};
#pragma unroll
      for (int u = 0; u < 8; ++u) o = mfma(A[u], P[u], o);
      if (mlane) o = f32x4{mx, (lsum[0] + lsum[1]) + (lsum[2] + lsum[3]), 0.f, 0.f};
      if ((lane & 15) < 4 || mlane) st_g(rs, ooff, pack_g(o, tg));
    }
    STAMP(7);
    if (w >= 4) {
      if (ffn) {
        const int gi = 32 * (w - 4) + (lane & 31);
        const u32 ho = WS_H + ((hh * 8 + 4 * (lane >> 5)) * 128 + gi) * 16;
        const u32 off[4] = {ho, ho + 2048, ho + 4096, ho + 6144};
        v4u ga[4];
        SPINS(12, (gather<4, 2>(rs, off, ga, tg)));
        const f32x4 a = (val_g(ga[0]) + val_g(ga[1])) + (val_g(ga[2]) + val_g(ga[3]));
        float r[4];
#pragma unroll
        for (int q = 0; q < 4; ++q) {
          auto sw = __builtin_amdgcn_permlane32_swap(f2u(a[q]), f2u(a[q]), false, false);
          const u32 s0 = sw[0], s1 = sw[1];
          const float hv = rbf(u2f(s0) + u2f(s1));
          r[q] = hv * __builtin_amdgcn_rcpf(1.f + __builtin_amdgcn_exp2f(-hv * 1.4426950408889634f));
        }
        if (lane < 32) *(LAS v2u*)(L + L_XH + (gi >> 4) * 160 + (gi & 15) * 8) = v2u{pk2(r[0], r[1]), pk2(r[2], r[3])};
        lds_arrive(lbase, C_HI);
      }
      waitvm<0>();
      lds_arrive(lbase, C_W2);
      STAMP(8);
    } else {
      waitvm<0>();
      lds_arrive(lbase, C_W2);
      lds_wait<1>(L, C_W2, WO_LATE == 1 ? 4 : 8);
      const int nt = w;
      const u32 a0 = tr_row + (((u32)nt ^ trf) * 32);
      v4u A[4];
#pragma unroll
      for (int t = 0; t < 4; ++t) A[t] = tr_frag(L, L_W + a0 + t * 4096, L_W + a0 + t * 4096 + 512);
      f32x4 acc = {0.f, 0.f, 0.f, 0.f};
      if (ffn) {
        v4u A2[2];
#pragma unroll
        for (int t = 0; t < 2; ++t) A2[t] = tr_frag(L, L_W1 + a0 + t * 4096, L_W1 + a0 + t * 4096 + 512);
        lds_wait<1>(L, C_HI, 4);
#pragma unroll
        for (int t = 0; t < 2; ++t) acc = mfma(lds128(L, L_XH + (lane & 7) * 160 + (4 * t + g) * 16), A2[t], acc);
      }
      u32 foff = WS_F + ((((ns * 4 + nt) * 2 + g) * 32 + hh) * 16 + (lane & 15)) * 16;
      u32 xqa = L_XQ + (lane & 7) * 288 + g * 16;
      asm volatile("" : "+v"(foff), "+v"(xqa));
      wg_barrier();
      STAMP(8);
      v4u B[4];
#pragma unroll
      for (int t = 0; t < 4; ++t) B[t] = lds128(L, xqa + t * 64);
      __builtin_amdgcn_sched_barrier(0);
#pragma unroll
      for (int t = 0; t < 4; ++t) acc = mfma(B[t], A[t], acc);
      if (lane < 32) st_g(rs, foff, pack_g(acc, tg));
      STAMP(9);
    }
    STAMPS_OUT();
    __builtin_amdgcn_s_waitcnt(0);
  } else {
    const int w = wave;
    const __amdgpu_buffer_rsrc_t rs = __builtin_amdgcn_make_buffer_rsrc((void*)p.ws, 0, (int)WS_END, 0x00020000);
    u32 tl0 = 0, tl1 = 0;
    if (TLBW) {
      const char* tb = w == 0 ? p.ws + WS_END
                     : w == 1 ? p.vc + ((((size_t)bb * HKV + xx) * SK + (SK - WINDOW) + sp * 256) * DH) * 2
                     : w == 2 ? p.wo + ((size_t)(4 * xx * 128 + 32 * db) * DM + ns * 64) * 2
                              : p.w2 + ((size_t)((ffn ? hh : 0) * 64) * DM + ns * 64) * 2;
      const u32 tstep = w == 2 ? 8192u : 4096u;
      const u32 tmax = w == 0 ? 0u : w == 1 ? 65472u : w == 2 ? 3 * 131072u + 31 * 1024u : 63 * 1024u;
      const u32 to = min((u32)lane * tstep, tmax), zero = 0;
      asm volatile("global_load_dword %0, %2, %3\n\tglobal_load_dword %1, %4, %5" : "=&v"(tl0), "=&v"(tl1) : "v"(to), "s"(tb), "v"(zero), "s"(p.out) : "memory");
    }
    asm volatile("s_waitcnt vmcnt(%2)" : "+v"(xv[0]), "+v"(xv[1]) : "i"(TLBW ? 5 : 3) : "memory");
    STAMP(11);
    {
      float v[2][8], s1[2], s2[2], piv[2];
#pragma unroll
      for (int r = 0; r < 2; ++r) {
#pragma unroll
        for (int q = 0; q < 4; ++q) { v[r][2 * q] = bflo(xv[r][q]); v[r][2 * q + 1] = bfhi(xv[r][q]); }
        piv[r] = u2f(__builtin_amdgcn_readfirstlane(f2u(v[r][0])));
        float d[8];
#pragma unroll
        for (int q = 0; q < 8; ++q) d[q] = v[r][q] - piv[r];
        s1[r] = ((d[0] + d[1]) + (d[2] + d[3])) + ((d[4] + d[5]) + (d[6] + d[7]));
        s2[r] = fmaf(d[1], d[1], d[0] * d[0]) + fmaf(d[3], d[3], d[2] * d[2]) + (fmaf(d[5], d[5], d[4] * d[4]) + fmaf(d[7], d[7], d[6] * d[6]));
      }
#define RED4(C, RM) { const float a = dppz<C, RM>(s1[0]), b = dppz<C, RM>(s2[0]), c = dppz<C, RM>(s1[1]), d = dppz<C, RM>(s2[1]); s1[0] += a; s2[0] += b; s1[1] += c; s2[1] += d; }
      RED4(0x111, 0xf) RED4(0x112, 0xf) RED4(0x114, 0xf) RED4(0x118, 0xf) RED4(0x142, 0xa) RED4(0x143, 0xc)
#undef RED4
#pragma unroll
      for (int r = 0; r < 2; ++r) {
        const float t1 = u2f(__builtin_amdgcn_readlane(f2u(s1[r]), 63)), t2 = u2f(__builtin_amdgcn_readlane(f2u(s2[r]), 63));
        const float md = t1 * (1.f / DM);
        const float var = rbf(fmaxf(fmaf(-md, md, t2 * (1.f / DM)), 0.f));
        const float mean = rbf(piv[r] + md);
        const float den = rbf(__builtin_amdgcn_sqrtf(rbf(var + 1e-5f)));
        const float rd = __builtin_amdgcn_rcpf(den);
        v4u xn;
#pragma unroll
        for (int q = 0; q < 4; ++q) {
          const u32 d2 = pk2(v[r][2 * q] - mean, v[r][2 * q + 1] - mean);
          xn[q] = pk2(bflo(d2) * rd, bfhi(d2) * rd);
        }
        if ((lane >> 4) == ks) *(LAS v4u*)(L + L_XQ + (2 * w + r) * 288 + (lane & 15) * 16) = xn;
        if ((lane >> 3) == ns) *(LAS v4u*)(L + L_XH + (2 * w + r) * 160 + (lane & 7) * 16) = xn;
      }
    }
    asm volatile("s_waitcnt vmcnt(%1)" : "+v"(ev) : "i"(TLBW ? 4 : 2) : "memory");
    const u64 e = (((u64)(u32)__builtin_amdgcn_readfirstlane(ev[1]) << 32) | (u32)__builtin_amdgcn_readfirstlane(ev[0])) + 1;
    const Tag tg = make_tag(e);
    if (w == 0 && lane == 0) *(LAS v4u*)(L + L_TAG) = v4u{tg.t0, tg.t1, tg.t2, tg.t3};
    lds_arrive(lbase, C_A);
    STAMP(1);
    LAS char* T = L + L_TR + w * 512;
    const int tb = (lane >> 2) & 7, tG = lane & 3;
    const u32 a0 = tr_row + (((u32)w ^ trf) * 32);
    lds_wait<1>(L, C_A, 12);
    STAMP(2);
    {
      v4u A[4], B[4];
#pragma unroll
      for (int t = 0; t < 4; ++t) { A[t] = tr_frag(L, L_W + a0 + t * 4096, L_W + a0 + t * 4096 + 512); B[t] = lds128(L, L_XQ + (lane & 7) * 288 + (4 * t + g) * 16); }
      __builtin_amdgcn_sched_barrier(0);
      f32x4 acc = {0.f, 0.f, 0.f, 0.f};
#pragma unroll
      for (int t = 0; t < 4; ++t) acc = mfma(A[t], B[t], acc);
      if ((lane & 15) < 8) *(LAS f32x4*)(T + (lane & 7) * 64 + g * 16) = acc;
      const f32x4 tq = *(LAS f32x4*)(T + (lane & 31) * 16);
      if (lane < 32) st_g(rs, WS_Q + (((xx * 8 + tb) * 4 + ks) * 128 + cs * 16 + w * 4 + tG) * 16, pack_g(tq, tg));
    }
    lds_arrive(lbase, C_P);
    STAMP(3);
    if (ffn) {
      lds_wait<1>(L, C_W1, 8);
      v4u A[2], B[2];
#pragma unroll
      for (int t = 0; t < 2; ++t) { A[t] = tr_frag(L, L_W1 + a0 + t * 4096, L_W1 + a0 + t * 4096 + 512); B[t] = lds128(L, L_XH + (lane & 7) * 160 + (4 * t + g) * 16); }
      f32x4 acc = {0.f, 0.f, 0.f, 0.f};
#pragma unroll
      for (int t = 0; t < 2; ++t) acc = mfma(A[t], B[t], acc);
      if ((lane & 15) < 8) *(LAS f32x4*)(T + (lane & 7) * 64 + g * 16) = acc;
      const f32x4 th = *(LAS f32x4*)(T + (lane & 31) * 16);
      if (lane < 32) st_g(rs, WS_H + (((hh * 8 + ns) * 8 + tb) * 16 + w * 4 + tG) * 16, pack_g(th, tg));
    }
    lds_arrive(lbase, C_H);
    asm volatile("s_waitcnt vmcnt(0)" : "+v"(xres), "+v"(code), "+v"(tl0), "+v"(tl1)::"memory");
    if (w < 2) {
      lds_wait<2>(L, F_G, 1);
      const u32 qo = WS_Q + ((xx * 8 + bb) * 4 * 128 + w * 64 + lane) * 16;
      const u32 off[4] = {qo, qo + 2048, qo + 4096, qo + 6144};
      v4u ga[4];
      SPINS(12, (gather<4, GSLEEP>(rs, off, ga, tg)));
      const f32x4 a = (val_g(ga[0]) + val_g(ga[1])) + (val_g(ga[2]) + val_g(ga[3]));
      *(LAS v2u*)(L + L_QI + (2 * w + (lane >> 5)) * 288 + (lane & 31) * 8) = v2u{pk2(a[0], a[1]), pk2(a[2], a[3])};
      lds_arrive(lbase, C_Q);
    }
    STAMP(4);
    lds_wait<1>(L, C_Q, 2);
    lds_wait<1>(L, F_K + (2 * w) * 4, 1);
    lds_wait<1>(L, F_K + (2 * w + 1) * 4, 1);
    STAMP(5);
    {
      const u32 c16 = (u32)(lane & 15);
      v4u B[4], A0[2][4], A1[2][4];
#pragma unroll
      for (int t = 0; t < 4; ++t) B[t] = lds128(L, L_QI + (lane & 3) * 288 + (4 * t + g) * 16);
#pragma unroll
      for (int u = 0; u < 2; ++u) {
        const u32 ka = L_K + (2 * w + u) * 8192 + c16 * 256 + (((u32)g ^ c16) * 16);
#pragma unroll
        for (int t = 0; t < 4; ++t) { A0[u][t] = lds128(L, ka ^ (u32)(t << 6)); A1[u][t] = lds128(L, (ka ^ (u32)(t << 6)) + 4096); }
      }
      __builtin_amdgcn_sched_barrier(0);
      f32x4 sc[4];
#pragma unroll
      for (int u = 0; u < 4; ++u) sc[u] = f32x4{0.f, 0.f, 0.f, 0.f};
#pragma unroll
      for (int t = 0; t < 4; ++t) {
        sc[0] = mfma(A0[0][t], B[t], sc[0]); sc[1] = mfma(A1[0][t], B[t], sc[1]);
        sc[2] = mfma(A0[1][t], B[t], sc[2]); sc[3] = mfma(A1[1][t], B[t], sc[3]);
      }
      float s[16];
#pragma unroll
      for (int u = 0; u < 4; ++u) {
        const u32 a = pk2(sc[u][0], sc[u][1]), b = pk2(sc[u][2], sc[u][3]);
        const u32 c = pk2(bflo(a) * 0.08838834764831845f, bfhi(a) * 0.08838834764831845f);
        const u32 d = pk2(bflo(b) * 0.08838834764831845f, bfhi(b) * 0.08838834764831845f);
        s[4 * u] = bflo(c); s[4 * u + 1] = bfhi(c); s[4 * u + 2] = bflo(d); s[4 * u + 3] = bfhi(d);
      }
      float m = fmaxf(fmaxf(fmaxf(fmaxf(s[0], s[1]), fmaxf(s[2], s[3])), fmaxf(fmaxf(s[4], s[5]), fmaxf(s[6], s[7]))),
                      fmaxf(fmaxf(fmaxf(s[8], s[9]), fmaxf(s[10], s[11])), fmaxf(fmaxf(s[12], s[13]), fmaxf(s[14], s[15]))));
      m = x16_32_max(m);
      if (lane < 4) *(LAS float*)(L + L_MX + (lane * 4 + w) * 4) = m;
      lds_arrive(lbase, C_M);
      lds_wait<0>(L, C_M, 4);
      const f32x4 m0 = *(LAS f32x4*)(L + L_MX + (lane & 3) * 16);
      const float M = fmaxf(fmaxf(m0[0], m0[1]), fmaxf(m0[2], m0[3]));
      const float Ml = M * 1.4426950408889634f;
      float ex[16];
#pragma unroll
      for (int r = 0; r < 16; ++r) ex[r] = __builtin_amdgcn_exp2f(fmaf(s[r], 1.4426950408889634f, -Ml));
      float l = (((ex[0] + ex[1]) + (ex[2] + ex[3])) + ((ex[4] + ex[5]) + (ex[6] + ex[7]))) + (((ex[8] + ex[9]) + (ex[10] + ex[11])) + ((ex[12] + ex[13]) + (ex[14] + ex[15])));
      if ((lane & 15) < 4) {
#pragma unroll
        for (int u = 0; u < 2; ++u)
          *(LAS v4u*)(L + L_K + (2 * w + u) * 8192 + 4096 + g * 64 + (lane & 3) * 16) =
              v4u{pk2(ex[8 * u], ex[8 * u + 1]), pk2(ex[8 * u + 2], ex[8 * u + 3]), pk2(ex[8 * u + 4], ex[8 * u + 5]), pk2(ex[8 * u + 6], ex[8 * u + 7])};
      }
      l = x16_32_sum(l);
      if (lane < 4) { *(LAS float*)(L + L_ML + (lane * 4 + w) * 4) = l; if (w == 0) *(LAS float*)(L + L_ML + 64 + lane * 4) = M; }
      lds_arrive(lbase, C_PP);
    }
    STAMP(6);
    wg_barrier();
    STAMP(7);
    {
      const u32 oo = WS_O + (((xx * 8 + 2 * w + (lane >> 5)) * 4) * 128 + db * 32 + (lane & 31)) * 16;
      const u32 off[4] = {oo, oo + 2048, oo + 4096, oo + 6144};
      v4u go[4];
      if (w == 0) {
        const u32 mo = WS_ML + (xx * 128 + lane) * 16;
        const u32 moff[2] = {mo, mo + 1024};
        v4u gm[2];
        __builtin_amdgcn_s_sleep(MSLEEP0);
        SPINS(13, (gather<2, MSLEEP>(rs, moff, gm, tg)));
        lds_flag(lbase, F_ML);
#pragma unroll
        for (int s = 0; s < 4; ++s) go[s] = ld_g(rs, off[s]);
#pragma unroll
        for (int r = 0; r < 2; ++r) {
          const float ms = u2f(gm[r][0] & 0xfffff000u), ls = u2f(gm[r][1] & 0xfffff000u);
          float M = fmaxf(ms, dppm<0x124>(ms));
          M = fmaxf(M, dppm<0x128>(M));
          const float ew = __builtin_amdgcn_exp2f((ms - M) * 1.4426950408889634f);
          float Ls = ew * ls;
          Ls += dppm<0x124>(Ls);
          Ls += dppm<0x128>(Ls);
          *(LAS float*)(L + L_WT + (((4 * r + g) * 4 + (lane & 3)) * 4 + ((lane >> 2) & 3)) * 4) = ew * __builtin_amdgcn_rcpf(Ls);
        }
        lds_arrive(lbase, F_ML);
      } else {
        lds_wait<0>(L, F_ML, 1);
#pragma unroll
        for (int s = 0; s < 4; ++s) go[s] = ld_g(rs, off[s]);
      }
      STAMP(8);
      {
        u32 acc0 = chk(chk(0u, go[0], tg), go[1], tg), acc1 = chk(chk(0u, go[2], tg), go[3], tg);
        if (!__all(((acc0 | acc1) & 0xfffu) == 0u)) (void)gather<4, 0>(rs, off, go, tg);
      }
      lds_wait<0>(L, F_ML, 2);
      const f32x4 wt = *(LAS f32x4*)(L + L_WT + ((2 * w + (lane >> 5)) * 4 + (lane & 3)) * 16);
      const f32x4 O = (val_g(go[0]) * wt[0] + val_g(go[1]) * wt[1]) + (val_g(go[2]) * wt[2] + val_g(go[3]) * wt[3]);
      *(LAS v2u*)(L + L_XQ + (2 * w + (lane >> 5)) * 288 + ((lane & 3) * 32 + ((lane >> 2) & 7) * 4) * 2) = v2u{pk2(O[0], O[1]), pk2(O[2], O[3])};
      STAMP(9);
      wg_barrier();
    }
    if (w == 0) {
      const u32 fo = WS_F + ((((ns * 4 + fnt) * 2 + fG) * 32 + (lane >> 2)) * 16 + 4 * fcq + (lane & 3)) * 16;
      const u32 off[2] = {fo, fo + 16 * 256};
      v4u gg[2];
      __builtin_amdgcn_s_sleep(FSLEEP0);
      SPINS(14, (gather<2, FSLEEP>(rs, off, gg, tg)));
      const f32x4 v = val_g(gg[0]) + val_g(gg[1]);
      auto a02 = __builtin_amdgcn_permlane32_swap(f2u(v[0]), f2u(v[2]), false, false);
      auto a13 = __builtin_amdgcn_permlane32_swap(f2u(v[1]), f2u(v[3]), false, false);
      const u32 p0 = a02[0], p1 = a02[1], p2 = a13[0], p3 = a13[1];
      const float lo = u2f(p0) + u2f(p1), hi = u2f(p2) + u2f(p3);
      auto a = __builtin_amdgcn_permlane16_swap(f2u(lo), f2u(hi), false, false);
      const u32 q0 = a[0], q1 = a[1];
      float t = u2f(q0) + u2f(q1);
      t += dppm<0x124>(t);
      t += dppm<0x128>(t);
      const u32 ob = pk2(bflo(xres) + t, 0.f);
      if ((lane & 12) == 0) {
        *(GAS unsigned short*)(p.out + (fb * DM + fcol) * 2) = (unsigned short)ob;
      }
      if (wg == 0 && lane == 0) __hip_atomic_store((GAS u64*)(p.ws + WS_EPOCH), e, __ATOMIC_RELAXED, __HIP_MEMORY_SCOPE_AGENT);
      STAMP(15);
    }
    STAMPS_OUT();
    __builtin_amdgcn_s_waitcnt(0);
  }
}

}

extern "C" void kernel_launch(void* const* d_in, const int* in_sizes, int n_in, void* d_out, int out_size, void* d_ws,
                              size_t ws_size, hipStream_t stream) {
  (void)in_sizes; (void)n_in; (void)out_size; (void)ws_size;
  Params p;
  p.x = (const char*)d_in[0];
  p.kc = (const char*)d_in[1];
  p.vc = (const char*)d_in[2];
  p.wq = (const char*)d_in[3];
  p.wo = (const char*)d_in[4];
  p.w1 = (const char*)d_in[6];
  p.w2 = (const char*)d_in[7];
  p.out = (char*)d_out;
  p.ws = (char*)d_ws;
  hipLaunchKernelGGL(fused_decode_kernel, dim3(NWG), dim3(NTHR), 0, stream, p);
}
```
